```python
import torch

def _softmax(x, axis=-1):
    return torch.softmax(x, dim=axis)
def _gelu(x):
    return torch.nn.functional.gelu(x, approximate="tanh")
def _silu(x):
    return torch.nn.functional.silu(x)
def _sigmoid(x):
    return torch.sigmoid(x)
def _layernorm(x, axis=-1, eps=1e-5):
    m = x.mean(dim=axis, keepdim=True)
    v = x.var(dim=axis, keepdim=True, unbiased=False)
    return (x - m) / torch.sqrt(v + eps)
def _scatter_add(tgt, idx, src, axis=0):
    return tgt.clone().index_add_(axis, idx, src)
def _segment_sum(src, seg, n):
    out = torch.zeros((n,) + src.shape[1:], dtype=src.dtype)
    return out.index_add_(0, seg, src)
def _pad(x, pw):
    flat = tuple(v for pair in reversed(pw) for v in pair)
    return torch.nn.functional.pad(x, flat)

kernel_name = 'standalone__fused_io_aware_attn__prompt'

def setup_inputs():

    _w = (lambda a: (lambda t: t.to(torch.bfloat16) if t.is_floating_point() else t)(torch.as_tensor(a)))
    B, H, S = 2, 32, 2048
    Dh = max(1, 1024 // 32)
    return {
        "q": _w((lambda *s: torch.randn(*s, dtype=torch.bfloat16) * 0.8)(B, H, S, Dh)),
        "k": _w((lambda *s: torch.randn(*s, dtype=torch.bfloat16) * 0.8)(B, H, S, Dh)),
        "v": _w((lambda *s: torch.randn(*s, dtype=torch.bfloat16) * 0.05)(B, H, S, Dh)),
    }

def reference(q, k, v):
    scale = q.shape[-1] ** -0.5
    s = torch.einsum('bhqd,bhkd->bhqk', q, k) * scale
    p = _softmax(s, axis=-1)
    return torch.einsum('bhqk,bhkd->bhqd', p, v)
```

```cpp
#include <hip/hip_runtime.h>
#include <hip/hip_bf16.h>
#include <stdint.h>

typedef float    f32x16 __attribute__((ext_vector_type(16)));
typedef float    f32x2  __attribute__((ext_vector_type(2)));
typedef __bf16   bf16x8 __attribute__((ext_vector_type(8)));
typedef __bf16   bf16x2 __attribute__((ext_vector_type(2)));
typedef unsigned u32x4  __attribute__((ext_vector_type(4)));
typedef short    s16x4  __attribute__((ext_vector_type(4)));

constexpr int SEQ = 2048, DH = 32;
constexpr int LDS_V = 32768, LDS_FB = 2 * LDS_V, LDS_CNT = LDS_FB + 8 * 4096, LDS_BYTES = LDS_CNT + 1280;
constexpr float CSCALE = 0.17677669529663687f * 1.4426950408889634f;

__device__ __forceinline__ unsigned cvtpk(float lo, float hi) {
  f32x2 v = {lo, hi}; bf16x2 b = __builtin_convertvector(v, bf16x2); return __builtin_bit_cast(unsigned, b);
}
typedef __attribute__((address_space(3))) char* lds_ptr;
__device__ __forceinline__ void store_tile(const f32x16& o, unsigned short* base, int lane) {
  unsigned w[8];
#pragma unroll
  for (int i = 0; i < 8; ++i) w[i] = cvtpk(o[2 * i], o[2 * i + 1]);
  u32x4 X, Y;
  { auto x = __builtin_amdgcn_permlane32_swap(w[0], w[2], false, false); auto y = __builtin_amdgcn_permlane32_swap(w[1], w[3], false, false); X = (u32x4){x[0], y[0], x[1], y[1]}; }
  { auto x = __builtin_amdgcn_permlane32_swap(w[4], w[6], false, false); auto y = __builtin_amdgcn_permlane32_swap(w[5], w[7], false, false); Y = (u32x4){x[0], y[0], x[1], y[1]}; }
#pragma unroll
  for (int i = 0; i < 4; ++i) { auto s = __builtin_amdgcn_permlane16_swap(X[i], Y[i], false, false); X[i] = s[0]; Y[i] = s[1]; }
  unsigned short* rowp = base + (lane & 15) * DH + (((lane >> 4) & 1) * 2 + (lane >> 5)) * 8;
  __builtin_nontemporal_store(X, (u32x4*)rowp);
  __builtin_nontemporal_store(Y, (u32x4*)(rowp + 16 * DH));
}
#define MFMA32(a, b, c) __builtin_amdgcn_mfma_f32_32x32x16_bf16(a, b, c, 0, 0, 0)
typedef short v4i16_t __attribute__((ext_vector_type(4)));

__device__ __forceinline__ void fallback_block(const unsigned short* Qr, const unsigned short* Kh, const unsigned short* Vh, unsigned short* Or, lds_ptr stg, int lane) {
  const int r32 = lane & 31, hi = lane >> 5;
  const bf16x8 q0 = *(const bf16x8*)(Qr + r32 * DH + hi * 8), q1 = *(const bf16x8*)(Qr + r32 * DH + 16 + hi * 8);
  float m = -INFINITY, l = 0.f; f32x16 o = {};
  const lds_ptr vp = stg + ((lane >> 4) & 1) * 32 + (lane & 3) * 8 + (4 * hi + ((lane & 15) >> 2)) * 64;
#pragma unroll 1
  for (int t = 0; t < SEQ / 32; ++t) {
    const unsigned short* kp = Kh + (t * 32 + r32) * DH + hi * 8;
    const bf16x8 k0 = *(const bf16x8*)kp, k1 = *(const bf16x8*)(kp + 16);
    const u32x4 va = *(const u32x4*)(Vh + t * 32 * DH + lane * 8), vb = *(const u32x4*)(Vh + t * 32 * DH + 512 + lane * 8);
    *(__attribute__((address_space(3))) u32x4*)(stg + lane * 16) = va;
    *(__attribute__((address_space(3))) u32x4*)(stg + 1024 + lane * 16) = vb;
    f32x16 s = {};
    s = MFMA32(k0, q0, s); s = MFMA32(k1, q1, s);
    float mx = s[0];
#pragma unroll
    for (int i = 1; i < 16; ++i) mx = fmaxf(mx, s[i]);
    { auto rr = __builtin_amdgcn_permlane32_swap(__float_as_uint(mx), __float_as_uint(mx), false, false); mx = fmaxf(__uint_as_float(rr[0]), __uint_as_float(rr[1])); }
    const float mn = fmaxf(m, mx * CSCALE), ms = (mn == -INFINITY) ? 0.f : mn;
    const float al = __builtin_amdgcn_exp2f(m - ms);
    m = mn; l *= al;
    unsigned pw[8];
#pragma unroll
    for (int i = 0; i < 8; ++i) {
      const float p0 = __builtin_amdgcn_exp2f(__builtin_fmaf(s[2 * i], CSCALE, -ms)), p1 = __builtin_amdgcn_exp2f(__builtin_fmaf(s[2 * i + 1], CSCALE, -ms));
      l += p0 + p1; pw[i] = cvtpk(p0, p1);
    }
#pragma unroll
    for (int i = 0; i < 16; ++i) o[i] *= al;
    __builtin_amdgcn_wave_barrier();
    asm volatile("s_waitcnt lgkmcnt(0)" ::: "memory");
    s16x4 vf[4];
#pragma unroll
    for (int i = 0; i < 4; ++i) vf[i] = __builtin_bit_cast(s16x4, __builtin_amdgcn_ds_read_tr16_b64_v4i16((__attribute__((address_space(3))) v4i16_t*)(vp + (i >> 1) * 1024 + (i & 1) * 512)));
    const bf16x8 v0 = __builtin_bit_cast(bf16x8, __builtin_shufflevector(vf[0], vf[1], 0, 1, 2, 3, 4, 5, 6, 7));
    const bf16x8 v1 = __builtin_bit_cast(bf16x8, __builtin_shufflevector(vf[2], vf[3], 0, 1, 2, 3, 4, 5, 6, 7));
    const u32x4 pa = {pw[0], pw[1], pw[2], pw[3]}, pb = {pw[4], pw[5], pw[6], pw[7]};
    o = MFMA32(v0, __builtin_bit_cast(bf16x8, pa), o); o = MFMA32(v1, __builtin_bit_cast(bf16x8, pb), o);
    asm volatile("s_waitcnt lgkmcnt(0)" ::: "memory");
    __builtin_amdgcn_wave_barrier();
  }
  { auto rr = __builtin_amdgcn_permlane32_swap(__float_as_uint(l), __float_as_uint(l), false, false); l = __uint_as_float(rr[0]) + __uint_as_float(rr[1]); }
  const float rl = 1.0f / l;
#pragma unroll
  for (int i = 0; i < 16; ++i) o[i] *= rl;
  store_tile(o, Or, lane);
}

__global__ __launch_bounds__(512, 1) void attn_d32_kernel(const unsigned short* __restrict__ Q, const unsigned short* __restrict__ K, const unsigned short* __restrict__ V, unsigned short* __restrict__ O) {
  __shared__ __attribute__((aligned(1024))) char lds[LDS_BYTES];
  const int tid0 = threadIdx.x;
  const unsigned lds0 = (unsigned)(uintptr_t)lds;
  int tid; unsigned bad;
  asm volatile(
      "s_load_dwordx8 s[44:51], %[ka], 0\n"
      "s_setprio 3\n"
      "s_mov_b32 s40, m0\n"
      "v_and_b32 v218, 63, %[tid]\n"
      "v_readfirstlane_b32 s66, %[tid]\n"
      "s_and_b32 s68, %[bid], 7\n"
      "s_lshl_b32 s68, s68, 3\n"
      "s_lshr_b32 s69, %[bid], 5\n"
      "s_add_u32 s68, s68, s69\n"
      "s_lshl_b32 s68, s68, 17\n"
      "s_lshl_b32 s65, %[bid], 12\n"
      "s_and_b32 s65, s65, 0x18000\n"
      "s_lshl_b32 s67, s66, 6\n"
      "s_and_b32 s67, s67, 0x7000\n"
      "s_add_u32 s65, s65, s67\n"
      "v_and_b32 v212, 31, v218\n"
      "v_lshlrev_b32 v212, 6, v212\n"
      "v_lshrrev_b32 v204, 5, v218\n"
      "v_lshl_or_b32 v212, v204, 4, v212\n"
      "v_and_b32 v252, 15, v218\n"
      "v_lshlrev_b32 v252, 6, v252\n"
      "v_and_b32 v206, 48, v218\n"
      "v_or_b32 v252, v252, v206\n"
      "v_lshlrev_b32 v253, 4, v218\n"
      "s_bitcmp1_b32 s66, 7\n"
      "s_cselect_b32 s64, 0x8000, 0\n"
      "s_and_b32 s71, s66, 64\n"
      "s_lshl_b32 s71, s71, 6\n"
      "s_add_u32 s64, s64, s71\n"
      "s_add_u32 s64, s64, %[lds0]\n"
      "s_and_b32 s41, s66, 0xc0\n"
      "s_lshl_b32 s41, s41, 4\n"
      "s_add_u32 s69, s41, %[lds0]\n"
      "s_mov_b32 m0, s69\n"
      "s_mov_b32 s54, 0x20000\n"
      "s_mov_b32 s55, 0x20000\n"
      "s_mov_b32 s58, 0x20000\n"
      "s_mov_b32 s59, 0x20000\n"
      "s_mov_b32 s62, 0x20000\n"
      "s_mov_b32 s63, 0x20000\n"
      "s_mov_b32 s38, 0x20000\n"
      "s_mov_b32 s39, 0x20000\n"
      "v_lshlrev_b32 v215, 2, v218\n"
      "v_add_u32 v215, 98304, v215\n"
      "v_add_u32 v215, %[lds0], v215\n"
      "v_mov_b32 v209, 0\n"
      "s_waitcnt lgkmcnt(0)\n"
      "s_add_u32 s36, s48, s68\n"
      "s_addc_u32 s37, s49, 0\n"
      "s_and_b32 s37, s37, 0xffff\n"
      "s_add_u32 s52, s46, s68\n"
      "s_addc_u32 s53, s47, 0\n"
      "s_and_b32 s53, s53, 0xffff\n"
      "s_add_u32 s56, s44, s68\n"
      "s_addc_u32 s57, s45, 0\n"
      "s_and_b32 s57, s57, 0xffff\n"
      "s_bitcmp1_b32 s66, 8\n"
      "s_cbranch_scc1 LB0_%=\n"
      "buffer_load_dwordx4 v252, s[52:55], s41 offen lds\n"
      "LB0_%=:\n"
      "buffer_load_dwordx4 v[112:115], v212, s[56:59], s65 offen offset:0\n"
      "buffer_load_dwordx4 v[116:119], v212, s[56:59], s65 offen offset:32\n"
      "ds_write2st64_b32 v215, v209, v209 offset0:0 offset1:1\n"
      "ds_write2st64_b32 v215, v209, v209 offset0:2 offset1:3\n"
      "ds_write_b32 v215, v209 offset:1024\n"
      "s_waitcnt lgkmcnt(0)\n"
      "s_barrier\n"
      "s_bitcmp1_b32 s66, 8\n"
      "s_cbranch_scc0 LX1_%=\n"
      "s_add_u32 m0, s69, 4096\n"
      "s_add_u32 s41, s41, 4096\n"
      "buffer_load_dwordx4 v252, s[52:55], s41 offen lds\n"
      "s_add_u32 m0, s69, 32768\n"
      "s_sub_u32 s41, s41, 4096\n"
      "buffer_load_dwordx4 v253, s[36:39], s41 offen lds\n"
      "s_add_u32 m0, s69, 36864\n"
      "s_add_u32 s41, s41, 4096\n"
      "buffer_load_dwordx4 v253, s[36:39], s41 offen lds\n"
      "LX1_%=:\n"
      "v_and_b32 v204, 16, v218\n"
      "v_lshlrev_b32 v213, 6, v204\n"
      "v_and_b32 v205, 32, v218\n"
      "v_lshl_or_b32 v213, v205, 3, v213\n"
      "v_and_b32 v206, 15, v218\n"
      "v_lshl_or_b32 v213, v206, 4, v213\n"
      "v_add_u32 v213, %[lds0], v213\n"
      "v_lshlrev_b32 v214, 1, v204\n"
      "v_lshl_or_b32 v214, v205, 4, v214\n"
      "v_and_b32 v207, 3, v218\n"
      "v_lshl_or_b32 v214, v207, 3, v214\n"
      "v_and_b32 v207, 12, v218\n"
      "v_lshl_or_b32 v214, v207, 4, v214\n"
      "v_add_u32 v214, 32768, v214\n"
      "v_add_u32 v214, %[lds0], v214\n"
      "v_mov_b32 v216, 0xdddddddd\n"
      "v_mov_b32 v207, 0x3f803f80\n"
      "v_cmp_eq_u32 vcc, 0, v206\n"
      "v_cmp_eq_u32 s[30:31], 1, v206\n"
      "v_cmp_eq_u32 s[72:73], 0, v205\n"
      "v_mov_b32 v208, 1\n"
      "s_mov_b32 s43, 0\n"
      "v_cndmask_b32 v200, 0, v207, vcc\n"
      "v_cndmask_b32 v201, 0, v207, vcc\n"
      "v_cndmask_b32 v202, 0, v207, s[30:31]\n"
      "v_cndmask_b32 v203, 0, v207, s[30:31]\n"
      "v_cndmask_b32 v217, 0, -1.0, s[72:73]\n"
      "v_lshlrev_b32 v219, 6, v206\n"
      "v_lshl_or_b32 v219, v204, 1, v219\n"
      "v_lshrrev_b32 v205, 1, v205\n"
      "v_or_b32 v219, v219, v205\n"
      "s_add_u32 s60, s50, s68\n"
      "s_addc_u32 s61, s51, 0\n"
      "s_and_b32 s61, s61, 0xffff\n"
      "v_mov_b32 v160, 0\n"
      "v_mov_b32 v161, 0\n"
      "v_mov_b32 v162, 0\n"
      "v_mov_b32 v163, 0\n"
      "v_mov_b32 v164, 0\n"
      "v_mov_b32 v165, 0\n"
      "v_mov_b32 v166, 0\n"
      "v_mov_b32 v167, 0\n"
      "v_mov_b32 v168, 0\n"
      "v_mov_b32 v169, 0\n"
      "v_mov_b32 v170, 0\n"
      "v_mov_b32 v171, 0\n"
      "v_mov_b32 v172, 0\n"
      "v_mov_b32 v173, 0\n"
      "v_mov_b32 v174, 0\n"
      "v_mov_b32 v175, 0\n"
      "v_mov_b32 v176, 0\n"
      "v_mov_b32 v177, 0\n"
      "v_mov_b32 v178, 0\n"
      "v_mov_b32 v179, 0\n"
      "v_mov_b32 v180, 0\n"
      "v_mov_b32 v181, 0\n"
      "v_mov_b32 v182, 0\n"
      "v_mov_b32 v183, 0\n"
      "v_mov_b32 v184, 0\n"
      "v_mov_b32 v185, 0\n"
      "v_mov_b32 v186, 0\n"
      "v_mov_b32 v187, 0\n"
      "v_mov_b32 v188, 0\n"
      "v_mov_b32 v189, 0\n"
      "v_mov_b32 v190, 0\n"
      "v_mov_b32 v191, 0\n"
      "v_mov_b32 v192, 0\n"
      "v_mov_b32 v193, 0\n"
      "v_mov_b32 v194, 0\n"
      "v_mov_b32 v195, 0\n"
      "v_mov_b32 v196, 0\n"
      "v_mov_b32 v197, 0\n"
      "v_mov_b32 v198, 0\n"
      "v_mov_b32 v199, 0\n"
      "s_bitcmp1_b32 s66, 7\n"
      "s_cselect_b32 s52, s36, s52\n"
      "s_cselect_b32 s53, s37, s53\n"
      "s_cselect_b64 vcc, -1, 0\n"
      "v_cndmask_b32 v211, v252, v253, vcc\n"
      "s_mov_b32 s41, s71\n"
      "s_bitcmp1_b32 s66, 8\n"
      "s_cbranch_scc1 LX2_%=\n"
      "s_add_u32 s41, s41, 8192\n"
      "s_waitcnt vmcnt(2)\n"
      "ds_add_u32 v215, v208 offset:1024\n"
      "LX2_%=:\n"
      "ds_read_b32 v209, v215 offset:1024\n"
      "s_waitcnt lgkmcnt(0)\n"
      "v_cmp_le_u32 vcc, 4, v209\n"
      "s_cmp_lg_u64 vcc, 0\n"
      "s_cbranch_scc0 LSH0_%=\n"
      "LRH0_%=:\n"
      "ds_read_b128 v[220:223], v213 offset:0\n"
      "ds_read_b128 v[224:227], v213 offset:512\n"
      "ds_read_b128 v[228:231], v213 offset:2048\n"
      "ds_read_b128 v[232:235], v213 offset:2560\n"
      "s_bitcmp1_b32 s66, 8\n"
      "s_cbranch_scc0 LX3_%=\n"
      "s_waitcnt vmcnt(3)\n"
      "s_branch LY3_%=\n"
      "LX3_%=:\n"
      "s_waitcnt vmcnt(0)\n"
      "LY3_%=:\n"
      "v_lshlrev_b32 v48, 16, v112\n"
      "v_and_b32 v49, 0xffff0000, v112\n"
      "v_lshlrev_b32 v50, 16, v113\n"
      "v_and_b32 v51, 0xffff0000, v113\n"
      "v_lshlrev_b32 v52, 16, v114\n"
      "v_and_b32 v53, 0xffff0000, v114\n"
      "v_lshlrev_b32 v54, 16, v115\n"
      "v_and_b32 v55, 0xffff0000, v115\n"
      "v_lshlrev_b32 v56, 16, v116\n"
      "v_and_b32 v57, 0xffff0000, v116\n"
      "v_lshlrev_b32 v58, 16, v117\n"
      "v_and_b32 v59, 0xffff0000, v117\n"
      "v_lshlrev_b32 v60, 16, v118\n"
      "v_and_b32 v61, 0xffff0000, v118\n"
      "v_lshlrev_b32 v62, 16, v119\n"
      "v_and_b32 v63, 0xffff0000, v119\n"
      "v_mul_f32 v48, 0x3e8293ee, v48\n"
      "v_mul_f32 v49, 0x3e8293ee, v49\n"
      "v_mul_f32 v50, 0x3e8293ee, v50\n"
      "v_mul_f32 v51, 0x3e8293ee, v51\n"
      "v_mul_f32 v52, 0x3e8293ee, v52\n"
      "v_mul_f32 v53, 0x3e8293ee, v53\n"
      "v_mul_f32 v54, 0x3e8293ee, v54\n"
      "v_mul_f32 v55, 0x3e8293ee, v55\n"
      "v_mul_f32 v56, 0x3e8293ee, v56\n"
      "v_mul_f32 v57, 0x3e8293ee, v57\n"
      "v_mul_f32 v58, 0x3e8293ee, v58\n"
      "v_mul_f32 v59, 0x3e8293ee, v59\n"
      "v_mul_f32 v60, 0x3e8293ee, v60\n"
      "v_mul_f32 v61, 0x3e8293ee, v61\n"
      "v_mul_f32 v62, 0x3e8293ee, v62\n"
      "v_mul_f32 v63, 0x3e8293ee, v63\n"
      "v_cvt_pk_bf16_f32 v112, v48, v49\n"
      "v_cvt_pk_bf16_f32 v113, v50, v51\n"
      "v_cvt_pk_bf16_f32 v114, v52, v53\n"
      "v_cvt_pk_bf16_f32 v115, v54, v55\n"
      "v_cvt_pk_bf16_f32 v116, v56, v57\n"
      "v_cvt_pk_bf16_f32 v117, v58, v59\n"
      "v_cvt_pk_bf16_f32 v118, v60, v61\n"
      "v_cvt_pk_bf16_f32 v119, v62, v63\n"
      "s_waitcnt lgkmcnt(0)\n"
      "s_nop 0\n"
      "v_mfma_f32_32x32x16_bf16 v[16:31], v[220:223], v[112:115], 0\n"
      "v_mfma_f32_32x32x16_bf16 v[16:31], v[224:227], v[116:119], v[16:31]\n"
      "s_bitcmp1_b32 s66, 8\n"
      "s_cbranch_scc0 LX4_%=\n"
      "s_waitcnt vmcnt(0)\n"
      "ds_add_u32 v215, v208 offset:256\n"
      "s_branch LY4_%=\n"
      "LX4_%=:\n"
      "buffer_load_dwordx4 v[120:123], v212, s[56:59], s65 offen offset:2048\n"
      "buffer_load_dwordx4 v[124:127], v212, s[56:59], s65 offen offset:2080\n"
      "LY4_%=:\n"
      "s_nop 7\n"
      "v_max3_f32 v204, v16, v17, v18\n"
      "v_max3_f32 v205, v19, v20, v21\n"
      "v_max3_f32 v204, v204, v22, v23\n"
      "v_max3_f32 v205, v205, v24, v25\n"
      "v_max3_f32 v204, v204, v26, v27\n"
      "v_max3_f32 v205, v205, v28, v29\n"
      "v_max3_f32 v204, v204, v30, v31\n"
      "v_max_f32 v204, v204, v205\n"
      "v_mov_b32 v205, v204\n"
      "s_nop 1\n"
      "v_permlane32_swap_b32 v204, v205\n"
      "s_nop 1\n"
      "v_max_f32 v204, v204, v205\n"
      "s_nop 1\n"
      "v_mfma_f32_32x32x2_f32 v[128:143], v217, v204, 0\n"
      "ds_read_b32 v209, v215 offset:256\n"
      "v_mfma_f32_32x32x16_bf16 v[16:31], v[220:223], v[112:115], v[128:143]\n"
      "v_mfma_f32_32x32x16_bf16 v[16:31], v[224:227], v[116:119], v[16:31]\n"
      "s_waitcnt lgkmcnt(0)\n"
      "v_cmp_le_u32 vcc, 4, v209\n"
      "v_mfma_f32_32x32x16_bf16 v[32:47], v[228:231], v[112:115], v[128:143]\n"
      "s_cmp_lg_u64 vcc, 0\n"
      "s_cselect_b32 s30, 1, 0\n"
      "v_mfma_f32_32x32x16_bf16 v[32:47], v[232:235], v[116:119], v[32:47]\n"
      "s_cbranch_scc0 LF1_%=\n"
      "ds_read_b128 v[80:83], v213 offset:4096\n"
      "ds_read_b128 v[88:91], v213 offset:6144\n"
      "ds_read_b128 v[84:87], v213 offset:4608\n"
      "ds_read_b128 v[92:95], v213 offset:6656\n"
      "ds_read_b64_tr_b16 v[96:97], v214 offset:0\n"
      "ds_read_b64_tr_b16 v[98:99], v214 offset:256\n"
      "ds_read_b64_tr_b16 v[100:101], v214 offset:1024\n"
      "ds_read_b64_tr_b16 v[102:103], v214 offset:1280\n"
      "ds_read_b64_tr_b16 v[104:105], v214 offset:2048\n"
      "ds_read_b64_tr_b16 v[106:107], v214 offset:2304\n"
      "ds_read_b64_tr_b16 v[108:109], v214 offset:3072\n"
      "ds_read_b64_tr_b16 v[110:111], v214 offset:3328\n"
      "LF1_%=:\n"
      "s_setprio 0\n"
      "s_nop 3\n"
      "v_exp_f32 v16, v16\n"
      "v_exp_f32 v17, v17\n"
      "v_exp_f32 v18, v18\n"
      "v_exp_f32 v19, v19\n"
      "s_setprio 1\n"
      "v_exp_f32 v20, v20\n"
      "v_exp_f32 v21, v21\n"
      "v_exp_f32 v22, v22\n"
      "v_exp_f32 v23, v23\n"
      "v_exp_f32 v24, v24\n"
      "v_exp_f32 v25, v25\n"
      "v_exp_f32 v26, v26\n"
      "v_exp_f32 v27, v27\n"
      "v_exp_f32 v28, v28\n"
      "v_exp_f32 v29, v29\n"
      "v_exp_f32 v30, v30\n"
      "v_exp_f32 v31, v31\n"
      "v_exp_f32 v32, v32\n"
      "v_exp_f32 v33, v33\n"
      "v_exp_f32 v34, v34\n"
      "v_exp_f32 v35, v35\n"
      "v_exp_f32 v36, v36\n"
      "v_exp_f32 v37, v37\n"
      "v_exp_f32 v38, v38\n"
      "v_exp_f32 v39, v39\n"
      "v_exp_f32 v40, v40\n"
      "v_exp_f32 v41, v41\n"
      "v_exp_f32 v42, v42\n"
      "v_exp_f32 v43, v43\n"
      "v_exp_f32 v44, v44\n"
      "v_exp_f32 v45, v45\n"
      "v_exp_f32 v46, v46\n"
      "v_exp_f32 v47, v47\n"
      "s_setprio 2\n"
      "s_bitcmp1_b32 s66, 8\n"
      "s_cbranch_scc0 LX5_%=\n"
      "buffer_load_dwordx4 v[120:123], v212, s[56:59], s65 offen offset:2048\n"
      "buffer_load_dwordx4 v[124:127], v212, s[56:59], s65 offen offset:2080\n"
      "LX5_%=:\n"
      "s_cmp_lg_u32 s30, 0\n"
      "s_cbranch_scc1 LF2_%=\n"
      "ds_read_b32 v209, v215 offset:256\n"
      "s_waitcnt lgkmcnt(0)\n"
      "v_cmp_le_u32 vcc, 4, v209\n"
      "s_cmp_lg_u64 vcc, 0\n"
      "s_cbranch_scc0 LSH1_%=\n"
      "LRH1_%=:\n"
      "ds_read_b128 v[80:83], v213 offset:4096\n"
      "ds_read_b128 v[88:91], v213 offset:6144\n"
      "ds_read_b128 v[84:87], v213 offset:4608\n"
      "ds_read_b128 v[92:95], v213 offset:6656\n"
      "ds_read_b64_tr_b16 v[96:97], v214 offset:0\n"
      "ds_read_b64_tr_b16 v[98:99], v214 offset:256\n"
      "ds_read_b64_tr_b16 v[100:101], v214 offset:1024\n"
      "ds_read_b64_tr_b16 v[102:103], v214 offset:1280\n"
      "ds_read_b64_tr_b16 v[104:105], v214 offset:2048\n"
      "ds_read_b64_tr_b16 v[106:107], v214 offset:2304\n"
      "ds_read_b64_tr_b16 v[108:109], v214 offset:3072\n"
      "ds_read_b64_tr_b16 v[110:111], v214 offset:3328\n"
      "LF2_%=:\n"
      "s_waitcnt lgkmcnt(0)\n"
      "v_mfma_f32_32x32x16_bf16 v[48:63], v[80:83], v[112:115], v[128:143]\n"
      "v_mfma_f32_32x32x16_bf16 v[64:79], v[88:91], v[112:115], v[128:143]\n"
      "ds_read_b64_tr_b16 v[236:237], v214 offset:4096\n"
      "v_mfma_f32_32x32x16_bf16 v[48:63], v[84:87], v[116:119], v[48:63]\n"
      "ds_read_b64_tr_b16 v[238:239], v214 offset:4352\n"
      "v_mfma_f32_32x32x16_bf16 v[64:79], v[92:95], v[116:119], v[64:79]\n"
      "v_smfmac_f32_32x32x32_bf16 v[160:175], v[96:99], v[16:23], v216\n"
      "ds_read_b64_tr_b16 v[240:241], v214 offset:5120\n"
      "v_smfmac_f32_16x16x64_bf16 v[192:195], v[200:203], v[16:23], v216\n"
      "ds_read_b64_tr_b16 v[242:243], v214 offset:5376\n"
      "v_smfmac_f32_32x32x32_bf16 v[160:175], v[100:103], v[24:31], v216\n"
      "v_smfmac_f32_16x16x64_bf16 v[192:195], v[200:203], v[24:31], v216\n"
      "ds_read_b64_tr_b16 v[244:245], v214 offset:6144\n"
      "v_smfmac_f32_32x32x32_bf16 v[160:175], v[104:107], v[32:39], v216\n"
      "ds_read_b64_tr_b16 v[246:247], v214 offset:6400\n"
      "v_smfmac_f32_16x16x64_bf16 v[192:195], v[200:203], v[32:39], v216\n"
      "v_smfmac_f32_32x32x32_bf16 v[160:175], v[108:111], v[40:47], v216\n"
      "ds_read_b64_tr_b16 v[248:249], v214 offset:7168\n"
      "v_smfmac_f32_16x16x64_bf16 v[192:195], v[200:203], v[40:47], v216\n"
      "ds_read_b64_tr_b16 v[250:251], v214 offset:7424\n"
      "ds_add_u32 v215, v208 offset:0\n"
      "s_bitcmp1_b32 s66, 8\n"
      "s_cbranch_scc0 LX6_%=\n"
      "s_add_u32 m0, s64, 8192\n"
      "s_add_u32 s41, s41, 8192\n"
      "buffer_load_dwordx4 v211, s[52:55], s41 offen lds\n"
      "buffer_load_dwordx4 v211, s[52:55], s41 offen offset:1024 lds\n"
      "buffer_load_dwordx4 v211, s[52:55], s41 offen offset:2048 lds\n"
      "buffer_load_dwordx4 v211, s[52:55], s41 offen offset:3072 lds\n"
      "s_add_u32 s41, s41, 2048\n"
      "s_add_u32 s64, s64, 2048\n"
      "LX6_%=:\n"
      "s_setprio 0\n"
      "v_exp_f32 v48, v48\n"
      "v_exp_f32 v49, v49\n"
      "v_exp_f32 v50, v50\n"
      "v_exp_f32 v51, v51\n"
      "s_setprio 1\n"
      "v_exp_f32 v52, v52\n"
      "v_exp_f32 v53, v53\n"
      "v_exp_f32 v54, v54\n"
      "v_exp_f32 v55, v55\n"
      "v_exp_f32 v56, v56\n"
      "v_exp_f32 v57, v57\n"
      "v_exp_f32 v58, v58\n"
      "v_exp_f32 v59, v59\n"
      "v_exp_f32 v60, v60\n"
      "v_exp_f32 v61, v61\n"
      "v_exp_f32 v62, v62\n"
      "v_exp_f32 v63, v63\n"
      "v_exp_f32 v64, v64\n"
      "v_exp_f32 v65, v65\n"
      "v_exp_f32 v66, v66\n"
      "v_exp_f32 v67, v67\n"
      "v_exp_f32 v68, v68\n"
      "v_exp_f32 v69, v69\n"
      "v_exp_f32 v70, v70\n"
      "v_exp_f32 v71, v71\n"
      "v_exp_f32 v72, v72\n"
      "v_exp_f32 v73, v73\n"
      "v_exp_f32 v74, v74\n"
      "v_exp_f32 v75, v75\n"
      "v_exp_f32 v76, v76\n"
      "v_exp_f32 v77, v77\n"
      "v_exp_f32 v78, v78\n"
      "v_exp_f32 v79, v79\n"
      "s_setprio 2\n"
      "s_waitcnt lgkmcnt(0)\n"
      "s_bitcmp1_b32 s66, 8\n"
      "s_cbranch_scc0 LX7_%=\n"
      "s_waitcnt vmcnt(4)\n"
      "s_branch LY7_%=\n"
      "LX7_%=:\n"
      "s_waitcnt vmcnt(0)\n"
      "LY7_%=:\n"
      "v_smfmac_f32_32x32x32_bf16 v[160:175], v[236:239], v[48:55], v216\n"
      "v_lshlrev_b32 v204, 16, v120\n"
      "v_and_b32 v205, 0xffff0000, v120\n"
      "v_mul_f32 v204, 0x3e8293ee, v204\n"
      "v_mul_f32 v205, 0x3e8293ee, v205\n"
      "v_cvt_pk_bf16_f32 v120, v204, v205\n"
      "v_lshlrev_b32 v206, 16, v121\n"
      "v_and_b32 v207, 0xffff0000, v121\n"
      "v_mul_f32 v206, 0x3e8293ee, v206\n"
      "v_smfmac_f32_16x16x64_bf16 v[192:195], v[200:203], v[48:55], v216\n"
      "v_mul_f32 v207, 0x3e8293ee, v207\n"
      "v_cvt_pk_bf16_f32 v121, v206, v207\n"
      "v_lshlrev_b32 v204, 16, v122\n"
      "v_and_b32 v205, 0xffff0000, v122\n"
      "v_mul_f32 v204, 0x3e8293ee, v204\n"
      "v_mul_f32 v205, 0x3e8293ee, v205\n"
      "v_cvt_pk_bf16_f32 v122, v204, v205\n"
      "v_lshlrev_b32 v206, 16, v123\n"
      "v_smfmac_f32_32x32x32_bf16 v[160:175], v[240:243], v[56:63], v216\n"
      "v_and_b32 v207, 0xffff0000, v123\n"
      "v_mul_f32 v206, 0x3e8293ee, v206\n"
      "v_mul_f32 v207, 0x3e8293ee, v207\n"
      "v_cvt_pk_bf16_f32 v123, v206, v207\n"
      "v_lshlrev_b32 v204, 16, v124\n"
      "v_and_b32 v205, 0xffff0000, v124\n"
      "v_mul_f32 v204, 0x3e8293ee, v204\n"
      "v_mul_f32 v205, 0x3e8293ee, v205\n"
      "v_smfmac_f32_16x16x64_bf16 v[192:195], v[200:203], v[56:63], v216\n"
      "v_cvt_pk_bf16_f32 v124, v204, v205\n"
      "v_lshlrev_b32 v206, 16, v125\n"
      "v_and_b32 v207, 0xffff0000, v125\n"
      "v_mul_f32 v206, 0x3e8293ee, v206\n"
      "v_mul_f32 v207, 0x3e8293ee, v207\n"
      "v_cvt_pk_bf16_f32 v125, v206, v207\n"
      "v_lshlrev_b32 v204, 16, v126\n"
      "v_and_b32 v205, 0xffff0000, v126\n"
      "v_smfmac_f32_32x32x32_bf16 v[160:175], v[244:247], v[64:71], v216\n"
      "v_mul_f32 v204, 0x3e8293ee, v204\n"
      "v_mul_f32 v205, 0x3e8293ee, v205\n"
      "v_cvt_pk_bf16_f32 v126, v204, v205\n"
      "v_lshlrev_b32 v206, 16, v127\n"
      "v_and_b32 v207, 0xffff0000, v127\n"
      "v_mul_f32 v206, 0x3e8293ee, v206\n"
      "v_mul_f32 v207, 0x3e8293ee, v207\n"
      "v_cvt_pk_bf16_f32 v127, v206, v207\n"
      "s_nop 1\n"
      "v_mfma_f32_32x32x16_bf16 v[144:159], v[220:223], v[120:123], 0\n"
      "v_mfma_f32_32x32x16_bf16 v[144:159], v[224:227], v[124:127], v[144:159]\n"
      "v_smfmac_f32_16x16x64_bf16 v[192:195], v[200:203], v[64:71], v216\n"
      "v_smfmac_f32_32x32x32_bf16 v[160:175], v[248:251], v[72:79], v216\n"
      "v_smfmac_f32_16x16x64_bf16 v[192:195], v[200:203], v[72:79], v216\n"
      "s_nop 8\n"
      "v_max3_f32 v206, v144, v145, v146\n"
      "v_max3_f32 v207, v147, v148, v149\n"
      "v_max3_f32 v206, v206, v150, v151\n"
      "v_max3_f32 v207, v207, v152, v153\n"
      "v_max3_f32 v206, v206, v154, v155\n"
      "v_max3_f32 v207, v207, v156, v157\n"
      "v_max3_f32 v206, v206, v158, v159\n"
      "v_max_f32 v206, v206, v207\n"
      "v_mov_b32 v207, v206\n"
      "s_nop 1\n"
      "v_permlane32_swap_b32 v206, v207\n"
      "s_nop 1\n"
      "v_max_f32 v206, v206, v207\n"
      "s_nop 1\n"
      "v_mfma_f32_32x32x2_f32 v[144:159], v217, v206, 0\n"
      "s_add_u32 m0, s64, 16384\n"
      "s_add_u32 s41, s41, 8192\n"
      "v_mfma_f32_32x32x16_bf16 v[16:31], v[220:223], v[120:123], v[144:159]\n"
      "buffer_load_dwordx4 v211, s[52:55], s41 offen lds\n"
      "v_mfma_f32_32x32x16_bf16 v[32:47], v[228:231], v[120:123], v[144:159]\n"
      "buffer_load_dwordx4 v211, s[52:55], s41 offen offset:1024 lds\n"
      "v_mfma_f32_32x32x16_bf16 v[16:31], v[224:227], v[124:127], v[16:31]\n"
      "v_mfma_f32_32x32x16_bf16 v[32:47], v[232:235], v[124:127], v[32:47]\n"
      "s_bitcmp1_b32 s66, 8\n"
      "s_cbranch_scc0 LX8_%=\n"
      "s_waitcnt vmcnt(2)\n"
      "ds_add_u32 v215, v208 offset:768\n"
      "s_nop 4\n"
      "s_branch LY8_%=\n"
      "LX8_%=:\n"
      "s_nop 7\n"
      "LY8_%=:\n"
      "s_setprio 0\n"
      "v_exp_f32 v16, v16\n"
      "v_exp_f32 v17, v17\n"
      "v_exp_f32 v18, v18\n"
      "v_exp_f32 v19, v19\n"
      "s_setprio 1\n"
      "v_exp_f32 v20, v20\n"
      "v_exp_f32 v21, v21\n"
      "v_exp_f32 v22, v22\n"
      "v_exp_f32 v23, v23\n"
      "v_exp_f32 v24, v24\n"
      "v_exp_f32 v25, v25\n"
      "v_exp_f32 v26, v26\n"
      "v_exp_f32 v27, v27\n"
      "v_exp_f32 v28, v28\n"
      "v_exp_f32 v29, v29\n"
      "v_exp_f32 v30, v30\n"
      "v_exp_f32 v31, v31\n"
      "v_exp_f32 v32, v32\n"
      "v_exp_f32 v33, v33\n"
      "v_exp_f32 v34, v34\n"
      "v_exp_f32 v35, v35\n"
      "v_exp_f32 v36, v36\n"
      "v_exp_f32 v37, v37\n"
      "v_exp_f32 v38, v38\n"
      "v_exp_f32 v39, v39\n"
      "v_exp_f32 v40, v40\n"
      "v_exp_f32 v41, v41\n"
      "v_exp_f32 v42, v42\n"
      "v_exp_f32 v43, v43\n"
      "v_exp_f32 v44, v44\n"
      "v_exp_f32 v45, v45\n"
      "v_exp_f32 v46, v46\n"
      "v_exp_f32 v47, v47\n"
      "s_setprio 2\n"
      "ds_read_b32 v209, v215 offset:768\n"
      "s_add_u32 s43, s43, 8\n"
      "s_add_u32 m0, s64, 24576\n"
      "s_add_u32 s41, s41, 8192\n"
      "v_mfma_f32_32x32x16_bf16 v[48:63], v[80:83], v[120:123], v[144:159]\n"
      "v_mfma_f32_32x32x16_bf16 v[64:79], v[88:91], v[120:123], v[144:159]\n"
      "v_mfma_f32_32x32x16_bf16 v[48:63], v[84:87], v[124:127], v[48:63]\n"
      "buffer_load_dwordx4 v211, s[52:55], s41 offen lds\n"
      "v_mfma_f32_32x32x16_bf16 v[64:79], v[92:95], v[124:127], v[64:79]\n"
      "v_smfmac_f32_32x32x32_bf16 v[176:191], v[96:99], v[16:23], v216\n"
      "v_smfmac_f32_16x16x64_bf16 v[196:199], v[200:203], v[16:23], v216\n"
      "buffer_load_dwordx4 v211, s[52:55], s41 offen offset:1024 lds\n"
      "s_waitcnt lgkmcnt(0)\n"
      "v_cmp_le_u32 vcc, 4, v209\n"
      "s_cmp_lg_u64 vcc, 0\n"
      "s_cbranch_scc0 LSH3_%=\n"
      "LRH3_%=:\n"
      "v_smfmac_f32_32x32x32_bf16 v[176:191], v[100:103], v[24:31], v216\n"
      "v_smfmac_f32_16x16x64_bf16 v[196:199], v[200:203], v[24:31], v216\n"
      "v_smfmac_f32_32x32x32_bf16 v[176:191], v[104:107], v[32:39], v216\n"
      "ds_read_b128 v[220:223], v213 offset:8192\n"
      "v_smfmac_f32_16x16x64_bf16 v[196:199], v[200:203], v[32:39], v216\n"
      "v_smfmac_f32_32x32x32_bf16 v[176:191], v[108:111], v[40:47], v216\n"
      "v_smfmac_f32_16x16x64_bf16 v[196:199], v[200:203], v[40:47], v216\n"
      "ds_read_b128 v[224:227], v213 offset:8704\n"
      "s_setprio 0\n"
      "v_exp_f32 v48, v48\n"
      "v_exp_f32 v49, v49\n"
      "v_exp_f32 v50, v50\n"
      "v_exp_f32 v51, v51\n"
      "s_setprio 1\n"
      "v_exp_f32 v52, v52\n"
      "v_exp_f32 v53, v53\n"
      "v_exp_f32 v54, v54\n"
      "v_exp_f32 v55, v55\n"
      "v_exp_f32 v56, v56\n"
      "v_exp_f32 v57, v57\n"
      "v_exp_f32 v58, v58\n"
      "v_exp_f32 v59, v59\n"
      "v_exp_f32 v60, v60\n"
      "v_exp_f32 v61, v61\n"
      "v_exp_f32 v62, v62\n"
      "v_exp_f32 v63, v63\n"
      "v_exp_f32 v64, v64\n"
      "v_exp_f32 v65, v65\n"
      "v_exp_f32 v66, v66\n"
      "v_exp_f32 v67, v67\n"
      "v_exp_f32 v68, v68\n"
      "v_exp_f32 v69, v69\n"
      "v_exp_f32 v70, v70\n"
      "v_exp_f32 v71, v71\n"
      "v_exp_f32 v72, v72\n"
      "v_exp_f32 v73, v73\n"
      "v_exp_f32 v74, v74\n"
      "v_exp_f32 v75, v75\n"
      "v_exp_f32 v76, v76\n"
      "v_exp_f32 v77, v77\n"
      "v_exp_f32 v78, v78\n"
      "v_exp_f32 v79, v79\n"
      "s_setprio 2\n"
      "s_waitcnt lgkmcnt(0)\n"
      "v_mfma_f32_32x32x16_bf16 v[16:31], v[220:223], v[112:115], v[128:143]\n"
      "v_mfma_f32_32x32x16_bf16 v[32:47], v[220:223], v[120:123], v[144:159]\n"
      "ds_read_b128 v[88:91], v213 offset:10240\n"
      "v_mfma_f32_32x32x16_bf16 v[16:31], v[224:227], v[116:119], v[16:31]\n"
      "v_mfma_f32_32x32x16_bf16 v[32:47], v[224:227], v[124:127], v[32:47]\n"
      "ds_read_b128 v[92:95], v213 offset:10752\n"
      "v_smfmac_f32_32x32x32_bf16 v[176:191], v[236:239], v[48:55], v216\n"
      "v_smfmac_f32_16x16x64_bf16 v[196:199], v[200:203], v[48:55], v216\n"
      "ds_read_b64_tr_b16 v[96:97], v214 offset:8192\n"
      "v_smfmac_f32_32x32x32_bf16 v[176:191], v[240:243], v[56:63], v216\n"
      "v_smfmac_f32_16x16x64_bf16 v[196:199], v[200:203], v[56:63], v216\n"
      "ds_read_b64_tr_b16 v[98:99], v214 offset:8448\n"
      "v_smfmac_f32_32x32x32_bf16 v[176:191], v[244:247], v[64:71], v216\n"
      "v_smfmac_f32_16x16x64_bf16 v[196:199], v[200:203], v[64:71], v216\n"
      "ds_read_b64_tr_b16 v[100:101], v214 offset:9216\n"
      "v_smfmac_f32_32x32x32_bf16 v[176:191], v[248:251], v[72:79], v216\n"
      "v_smfmac_f32_16x16x64_bf16 v[196:199], v[200:203], v[72:79], v216\n"
      "ds_read_b64_tr_b16 v[102:103], v214 offset:9472\n"
      "s_setprio 0\n"
      "s_mov_b32 s42, 4\n"
      ".p2align 6\n"
      "LL_%=:\n"
      "v_exp_f32 v16, v16\n"
      "v_exp_f32 v17, v17\n"
      "v_exp_f32 v18, v18\n"
      "v_exp_f32 v19, v19\n"
      "s_setprio 1\n"
      "v_exp_f32 v20, v20\n"
      "v_exp_f32 v21, v21\n"
      "v_exp_f32 v22, v22\n"
      "v_exp_f32 v23, v23\n"
      "v_exp_f32 v24, v24\n"
      "v_exp_f32 v25, v25\n"
      "v_exp_f32 v26, v26\n"
      "v_exp_f32 v27, v27\n"
      "v_exp_f32 v28, v28\n"
      "v_exp_f32 v29, v29\n"
      "v_exp_f32 v30, v30\n"
      "v_exp_f32 v31, v31\n"
      "v_exp_f32 v32, v32\n"
      "v_exp_f32 v33, v33\n"
      "v_exp_f32 v34, v34\n"
      "v_exp_f32 v35, v35\n"
      "v_exp_f32 v36, v36\n"
      "v_exp_f32 v37, v37\n"
      "v_exp_f32 v38, v38\n"
      "v_exp_f32 v39, v39\n"
      "v_exp_f32 v40, v40\n"
      "v_exp_f32 v41, v41\n"
      "v_exp_f32 v42, v42\n"
      "v_exp_f32 v43, v43\n"
      "v_exp_f32 v44, v44\n"
      "v_exp_f32 v45, v45\n"
      "v_exp_f32 v46, v46\n"
      "v_exp_f32 v47, v47\n"
      "s_setprio 2\n"
      "s_waitcnt lgkmcnt(0)\n"
      "v_mfma_f32_32x32x16_bf16 v[48:63], v[88:91], v[112:115], v[128:143]\n"
      "ds_read_b128 v[80:83], v213 offset:12288\n"
      "v_mfma_f32_32x32x16_bf16 v[64:79], v[88:91], v[120:123], v[144:159]\n"
      "ds_read_b128 v[84:87], v213 offset:12800\n"
      "v_mfma_f32_32x32x16_bf16 v[48:63], v[92:95], v[116:119], v[48:63]\n"
      "v_mfma_f32_32x32x16_bf16 v[64:79], v[92:95], v[124:127], v[64:79]\n"
      "ds_read_b64_tr_b16 v[104:105], v214 offset:10240\n"
      "v_smfmac_f32_32x32x32_bf16 v[160:175], v[96:99], v[16:23], v216\n"
      "v_smfmac_f32_32x32x32_bf16 v[176:191], v[96:99], v[32:39], v216\n"
      "ds_read_b64_tr_b16 v[106:107], v214 offset:10496\n"
      "v_smfmac_f32_32x32x32_bf16 v[160:175], v[100:103], v[24:31], v216\n"
      "ds_read_b64_tr_b16 v[108:109], v214 offset:11264\n"
      "v_smfmac_f32_32x32x32_bf16 v[176:191], v[100:103], v[40:47], v216\n"
      "v_smfmac_f32_16x16x64_bf16 v[192:195], v[200:203], v[16:23], v216\n"
      "ds_read_b64_tr_b16 v[110:111], v214 offset:11520\n"
      "v_smfmac_f32_16x16x64_bf16 v[196:199], v[200:203], v[32:39], v216\n"
      "v_smfmac_f32_16x16x64_bf16 v[192:195], v[200:203], v[24:31], v216\n"
      "s_waitcnt vmcnt(2)\n"
      "ds_add_u32 v215, v208\n"
      "v_smfmac_f32_16x16x64_bf16 v[196:199], v[200:203], v[40:47], v216\n"
      "s_setprio 0\n"
      "v_exp_f32 v48, v48\n"
      "v_exp_f32 v49, v49\n"
      "v_exp_f32 v50, v50\n"
      "v_exp_f32 v51, v51\n"
      "s_setprio 1\n"
      "v_exp_f32 v52, v52\n"
      "v_exp_f32 v53, v53\n"
      "v_exp_f32 v54, v54\n"
      "v_exp_f32 v55, v55\n"
      "v_exp_f32 v56, v56\n"
      "v_exp_f32 v57, v57\n"
      "v_exp_f32 v58, v58\n"
      "v_exp_f32 v59, v59\n"
      "v_exp_f32 v60, v60\n"
      "v_exp_f32 v61, v61\n"
      "v_exp_f32 v62, v62\n"
      "v_exp_f32 v63, v63\n"
      "v_exp_f32 v64, v64\n"
      "v_exp_f32 v65, v65\n"
      "v_exp_f32 v66, v66\n"
      "v_exp_f32 v67, v67\n"
      "v_exp_f32 v68, v68\n"
      "v_exp_f32 v69, v69\n"
      "v_exp_f32 v70, v70\n"
      "v_exp_f32 v71, v71\n"
      "v_exp_f32 v72, v72\n"
      "v_exp_f32 v73, v73\n"
      "v_exp_f32 v74, v74\n"
      "v_exp_f32 v75, v75\n"
      "v_exp_f32 v76, v76\n"
      "v_exp_f32 v77, v77\n"
      "v_exp_f32 v78, v78\n"
      "v_exp_f32 v79, v79\n"
      "s_setprio 2\n"
      "s_waitcnt lgkmcnt(0)\n"
      "v_mfma_f32_32x32x16_bf16 v[16:31], v[80:83], v[112:115], v[128:143]\n"
      "ds_read_b128 v[88:91], v213 offset:14336\n"
      "v_mfma_f32_32x32x16_bf16 v[32:47], v[80:83], v[120:123], v[144:159]\n"
      "ds_read_b128 v[92:95], v213 offset:14848\n"
      "v_mfma_f32_32x32x16_bf16 v[16:31], v[84:87], v[116:119], v[16:31]\n"
      "v_mfma_f32_32x32x16_bf16 v[32:47], v[84:87], v[124:127], v[32:47]\n"
      "ds_read_b64_tr_b16 v[96:97], v214 offset:12288\n"
      "v_smfmac_f32_32x32x32_bf16 v[160:175], v[104:107], v[48:55], v216\n"
      "v_smfmac_f32_32x32x32_bf16 v[176:191], v[104:107], v[64:71], v216\n"
      "ds_read_b64_tr_b16 v[98:99], v214 offset:12544\n"
      "v_smfmac_f32_32x32x32_bf16 v[160:175], v[108:111], v[56:63], v216\n"
      "ds_read_b64_tr_b16 v[100:101], v214 offset:13312\n"
      "v_smfmac_f32_32x32x32_bf16 v[176:191], v[108:111], v[72:79], v216\n"
      "v_smfmac_f32_16x16x64_bf16 v[192:195], v[200:203], v[48:55], v216\n"
      "ds_read_b64_tr_b16 v[102:103], v214 offset:13568\n"
      "v_smfmac_f32_16x16x64_bf16 v[196:199], v[200:203], v[64:71], v216\n"
      "v_smfmac_f32_16x16x64_bf16 v[192:195], v[200:203], v[56:63], v216\n"
      "v_smfmac_f32_16x16x64_bf16 v[196:199], v[200:203], v[72:79], v216\n"
      "s_setprio 0\n"
      "v_exp_f32 v16, v16\n"
      "v_exp_f32 v17, v17\n"
      "v_exp_f32 v18, v18\n"
      "v_exp_f32 v19, v19\n"
      "s_setprio 1\n"
      "v_exp_f32 v20, v20\n"
      "v_exp_f32 v21, v21\n"
      "v_exp_f32 v22, v22\n"
      "v_exp_f32 v23, v23\n"
      "v_exp_f32 v24, v24\n"
      "v_exp_f32 v25, v25\n"
      "v_exp_f32 v26, v26\n"
      "v_exp_f32 v27, v27\n"
      "v_exp_f32 v28, v28\n"
      "v_exp_f32 v29, v29\n"
      "v_exp_f32 v30, v30\n"
      "v_exp_f32 v31, v31\n"
      "v_exp_f32 v32, v32\n"
      "v_exp_f32 v33, v33\n"
      "v_exp_f32 v34, v34\n"
      "v_exp_f32 v35, v35\n"
      "v_exp_f32 v36, v36\n"
      "v_exp_f32 v37, v37\n"
      "v_exp_f32 v38, v38\n"
      "v_exp_f32 v39, v39\n"
      "v_exp_f32 v40, v40\n"
      "v_exp_f32 v41, v41\n"
      "v_exp_f32 v42, v42\n"
      "v_exp_f32 v43, v43\n"
      "v_exp_f32 v44, v44\n"
      "v_exp_f32 v45, v45\n"
      "v_exp_f32 v46, v46\n"
      "v_exp_f32 v47, v47\n"
      "s_setprio 2\n"
      "s_waitcnt lgkmcnt(0)\n"
      "ds_read_b32 v209, v215\n"
      "v_mfma_f32_32x32x16_bf16 v[48:63], v[88:91], v[112:115], v[128:143]\n"
      "s_add_u32 s43, s43, 8\n"
      "ds_read_b64_tr_b16 v[104:105], v214 offset:14336\n"
      "v_mfma_f32_32x32x16_bf16 v[64:79], v[88:91], v[120:123], v[144:159]\n"
      "ds_read_b64_tr_b16 v[106:107], v214 offset:14592\n"
      "s_add_u32 m0, s64, 0\n"
      "v_mfma_f32_32x32x16_bf16 v[48:63], v[92:95], v[116:119], v[48:63]\n"
      "ds_read_b64_tr_b16 v[108:109], v214 offset:15360\n"
      "s_add_u32 s41, s41, 8192\n"
      "v_mfma_f32_32x32x16_bf16 v[64:79], v[92:95], v[124:127], v[64:79]\n"
      "ds_read_b64_tr_b16 v[110:111], v214 offset:15616\n"
      "v_smfmac_f32_32x32x32_bf16 v[160:175], v[96:99], v[16:23], v216\n"
      "s_waitcnt lgkmcnt(4)\n"
      "v_cmp_le_u32 vcc, s43, v209\n"
      "s_cmp_lg_u64 vcc, 0\n"
      "s_cbranch_scc0 LS6_%=\n"
      "LR6_%=:\n"
      "v_smfmac_f32_32x32x32_bf16 v[176:191], v[96:99], v[32:39], v216\n"
      "ds_read_b128 v[80:83], v213 offset:16384\n"
      "v_smfmac_f32_32x32x32_bf16 v[160:175], v[100:103], v[24:31], v216\n"
      "ds_read_b128 v[84:87], v213 offset:16896\n"
      "v_smfmac_f32_32x32x32_bf16 v[176:191], v[100:103], v[40:47], v216\n"
      "buffer_load_dwordx4 v211, s[52:55], s41 offen lds\n"
      "v_smfmac_f32_16x16x64_bf16 v[192:195], v[200:203], v[16:23], v216\n"
      "v_smfmac_f32_16x16x64_bf16 v[196:199], v[200:203], v[32:39], v216\n"
      "buffer_load_dwordx4 v211, s[52:55], s41 offen offset:1024 lds\n"
      "v_smfmac_f32_16x16x64_bf16 v[192:195], v[200:203], v[24:31], v216\n"
      "v_smfmac_f32_16x16x64_bf16 v[196:199], v[200:203], v[40:47], v216\n"
      "s_setprio 0\n"
      "v_exp_f32 v48, v48\n"
      "v_exp_f32 v49, v49\n"
      "v_exp_f32 v50, v50\n"
      "v_exp_f32 v51, v51\n"
      "s_setprio 1\n"
      "v_exp_f32 v52, v52\n"
      "v_exp_f32 v53, v53\n"
      "v_exp_f32 v54, v54\n"
      "v_exp_f32 v55, v55\n"
      "v_exp_f32 v56, v56\n"
      "v_exp_f32 v57, v57\n"
      "v_exp_f32 v58, v58\n"
      "v_exp_f32 v59, v59\n"
      "v_exp_f32 v60, v60\n"
      "v_exp_f32 v61, v61\n"
      "v_exp_f32 v62, v62\n"
      "v_exp_f32 v63, v63\n"
      "v_exp_f32 v64, v64\n"
      "v_exp_f32 v65, v65\n"
      "v_exp_f32 v66, v66\n"
      "v_exp_f32 v67, v67\n"
      "v_exp_f32 v68, v68\n"
      "v_exp_f32 v69, v69\n"
      "v_exp_f32 v70, v70\n"
      "v_exp_f32 v71, v71\n"
      "v_exp_f32 v72, v72\n"
      "v_exp_f32 v73, v73\n"
      "v_exp_f32 v74, v74\n"
      "v_exp_f32 v75, v75\n"
      "v_exp_f32 v76, v76\n"
      "v_exp_f32 v77, v77\n"
      "v_exp_f32 v78, v78\n"
      "v_exp_f32 v79, v79\n"
      "s_setprio 2\n"
      "s_waitcnt lgkmcnt(0)\n"
      "v_mfma_f32_32x32x16_bf16 v[16:31], v[80:83], v[112:115], v[128:143]\n"
      "ds_read_b128 v[88:91], v213 offset:18432\n"
      "v_mfma_f32_32x32x16_bf16 v[32:47], v[80:83], v[120:123], v[144:159]\n"
      "ds_read_b128 v[92:95], v213 offset:18944\n"
      "v_mfma_f32_32x32x16_bf16 v[16:31], v[84:87], v[116:119], v[16:31]\n"
      "v_mfma_f32_32x32x16_bf16 v[32:47], v[84:87], v[124:127], v[32:47]\n"
      "ds_read_b64_tr_b16 v[96:97], v214 offset:16384\n"
      "v_smfmac_f32_32x32x32_bf16 v[160:175], v[104:107], v[48:55], v216\n"
      "v_smfmac_f32_32x32x32_bf16 v[176:191], v[104:107], v[64:71], v216\n"
      "ds_read_b64_tr_b16 v[98:99], v214 offset:16640\n"
      "v_smfmac_f32_32x32x32_bf16 v[160:175], v[108:111], v[56:63], v216\n"
      "ds_read_b64_tr_b16 v[100:101], v214 offset:17408\n"
      "v_smfmac_f32_32x32x32_bf16 v[176:191], v[108:111], v[72:79], v216\n"
      "v_smfmac_f32_16x16x64_bf16 v[192:195], v[200:203], v[48:55], v216\n"
      "ds_read_b64_tr_b16 v[102:103], v214 offset:17664\n"
      "v_smfmac_f32_16x16x64_bf16 v[196:199], v[200:203], v[64:71], v216\n"
      "v_smfmac_f32_16x16x64_bf16 v[192:195], v[200:203], v[56:63], v216\n"
      "v_smfmac_f32_16x16x64_bf16 v[196:199], v[200:203], v[72:79], v216\n"
      "s_setprio 0\n"
      "v_exp_f32 v16, v16\n"
      "v_exp_f32 v17, v17\n"
      "v_exp_f32 v18, v18\n"
      "v_exp_f32 v19, v19\n"
      "s_setprio 1\n"
      "v_exp_f32 v20, v20\n"
      "v_exp_f32 v21, v21\n"
      "v_exp_f32 v22, v22\n"
      "v_exp_f32 v23, v23\n"
      "v_exp_f32 v24, v24\n"
      "v_exp_f32 v25, v25\n"
      "v_exp_f32 v26, v26\n"
      "v_exp_f32 v27, v27\n"
      "v_exp_f32 v28, v28\n"
      "v_exp_f32 v29, v29\n"
      "v_exp_f32 v30, v30\n"
      "v_exp_f32 v31, v31\n"
      "v_exp_f32 v32, v32\n"
      "v_exp_f32 v33, v33\n"
      "v_exp_f32 v34, v34\n"
      "v_exp_f32 v35, v35\n"
      "v_exp_f32 v36, v36\n"
      "v_exp_f32 v37, v37\n"
      "v_exp_f32 v38, v38\n"
      "v_exp_f32 v39, v39\n"
      "v_exp_f32 v40, v40\n"
      "v_exp_f32 v41, v41\n"
      "v_exp_f32 v42, v42\n"
      "v_exp_f32 v43, v43\n"
      "v_exp_f32 v44, v44\n"
      "v_exp_f32 v45, v45\n"
      "v_exp_f32 v46, v46\n"
      "v_exp_f32 v47, v47\n"
      "s_setprio 2\n"
      "s_waitcnt lgkmcnt(0)\n"
      "v_mfma_f32_32x32x16_bf16 v[48:63], v[88:91], v[112:115], v[128:143]\n"
      "ds_read_b128 v[80:83], v213 offset:20480\n"
      "v_mfma_f32_32x32x16_bf16 v[64:79], v[88:91], v[120:123], v[144:159]\n"
      "ds_read_b128 v[84:87], v213 offset:20992\n"
      "v_mfma_f32_32x32x16_bf16 v[48:63], v[92:95], v[116:119], v[48:63]\n"
      "v_mfma_f32_32x32x16_bf16 v[64:79], v[92:95], v[124:127], v[64:79]\n"
      "ds_read_b64_tr_b16 v[104:105], v214 offset:18432\n"
      "v_smfmac_f32_32x32x32_bf16 v[160:175], v[96:99], v[16:23], v216\n"
      "v_smfmac_f32_32x32x32_bf16 v[176:191], v[96:99], v[32:39], v216\n"
      "ds_read_b64_tr_b16 v[106:107], v214 offset:18688\n"
      "v_smfmac_f32_32x32x32_bf16 v[160:175], v[100:103], v[24:31], v216\n"
      "ds_read_b64_tr_b16 v[108:109], v214 offset:19456\n"
      "v_smfmac_f32_32x32x32_bf16 v[176:191], v[100:103], v[40:47], v216\n"
      "v_smfmac_f32_16x16x64_bf16 v[192:195], v[200:203], v[16:23], v216\n"
      "ds_read_b64_tr_b16 v[110:111], v214 offset:19712\n"
      "v_smfmac_f32_16x16x64_bf16 v[196:199], v[200:203], v[32:39], v216\n"
      "v_smfmac_f32_16x16x64_bf16 v[192:195], v[200:203], v[24:31], v216\n"
      "s_waitcnt vmcnt(2)\n"
      "ds_add_u32 v215, v208\n"
      "v_smfmac_f32_16x16x64_bf16 v[196:199], v[200:203], v[40:47], v216\n"
      "s_setprio 0\n"
      "v_exp_f32 v48, v48\n"
      "v_exp_f32 v49, v49\n"
      "v_exp_f32 v50, v50\n"
      "v_exp_f32 v51, v51\n"
      "s_setprio 1\n"
      "v_exp_f32 v52, v52\n"
      "v_exp_f32 v53, v53\n"
      "v_exp_f32 v54, v54\n"
      "v_exp_f32 v55, v55\n"
      "v_exp_f32 v56, v56\n"
      "v_exp_f32 v57, v57\n"
      "v_exp_f32 v58, v58\n"
      "v_exp_f32 v59, v59\n"
      "v_exp_f32 v60, v60\n"
      "v_exp_f32 v61, v61\n"
      "v_exp_f32 v62, v62\n"
      "v_exp_f32 v63, v63\n"
      "v_exp_f32 v64, v64\n"
      "v_exp_f32 v65, v65\n"
      "v_exp_f32 v66, v66\n"
      "v_exp_f32 v67, v67\n"
      "v_exp_f32 v68, v68\n"
      "v_exp_f32 v69, v69\n"
      "v_exp_f32 v70, v70\n"
      "v_exp_f32 v71, v71\n"
      "v_exp_f32 v72, v72\n"
      "v_exp_f32 v73, v73\n"
      "v_exp_f32 v74, v74\n"
      "v_exp_f32 v75, v75\n"
      "v_exp_f32 v76, v76\n"
      "v_exp_f32 v77, v77\n"
      "v_exp_f32 v78, v78\n"
      "v_exp_f32 v79, v79\n"
      "s_setprio 2\n"
      "s_waitcnt lgkmcnt(0)\n"
      "v_mfma_f32_32x32x16_bf16 v[16:31], v[80:83], v[112:115], v[128:143]\n"
      "ds_read_b128 v[88:91], v213 offset:22528\n"
      "v_mfma_f32_32x32x16_bf16 v[32:47], v[80:83], v[120:123], v[144:159]\n"
      "ds_read_b128 v[92:95], v213 offset:23040\n"
      "v_mfma_f32_32x32x16_bf16 v[16:31], v[84:87], v[116:119], v[16:31]\n"
      "v_mfma_f32_32x32x16_bf16 v[32:47], v[84:87], v[124:127], v[32:47]\n"
      "ds_read_b64_tr_b16 v[96:97], v214 offset:20480\n"
      "v_smfmac_f32_32x32x32_bf16 v[160:175], v[104:107], v[48:55], v216\n"
      "v_smfmac_f32_32x32x32_bf16 v[176:191], v[104:107], v[64:71], v216\n"
      "ds_read_b64_tr_b16 v[98:99], v214 offset:20736\n"
      "v_smfmac_f32_32x32x32_bf16 v[160:175], v[108:111], v[56:63], v216\n"
      "ds_read_b64_tr_b16 v[100:101], v214 offset:21504\n"
      "v_smfmac_f32_32x32x32_bf16 v[176:191], v[108:111], v[72:79], v216\n"
      "v_smfmac_f32_16x16x64_bf16 v[192:195], v[200:203], v[48:55], v216\n"
      "ds_read_b64_tr_b16 v[102:103], v214 offset:21760\n"
      "v_smfmac_f32_16x16x64_bf16 v[196:199], v[200:203], v[64:71], v216\n"
      "v_smfmac_f32_16x16x64_bf16 v[192:195], v[200:203], v[56:63], v216\n"
      "v_smfmac_f32_16x16x64_bf16 v[196:199], v[200:203], v[72:79], v216\n"
      "s_setprio 0\n"
      "v_exp_f32 v16, v16\n"
      "v_exp_f32 v17, v17\n"
      "v_exp_f32 v18, v18\n"
      "v_exp_f32 v19, v19\n"
      "s_setprio 1\n"
      "v_exp_f32 v20, v20\n"
      "v_exp_f32 v21, v21\n"
      "v_exp_f32 v22, v22\n"
      "v_exp_f32 v23, v23\n"
      "v_exp_f32 v24, v24\n"
      "v_exp_f32 v25, v25\n"
      "v_exp_f32 v26, v26\n"
      "v_exp_f32 v27, v27\n"
      "v_exp_f32 v28, v28\n"
      "v_exp_f32 v29, v29\n"
      "v_exp_f32 v30, v30\n"
      "v_exp_f32 v31, v31\n"
      "v_exp_f32 v32, v32\n"
      "v_exp_f32 v33, v33\n"
      "v_exp_f32 v34, v34\n"
      "v_exp_f32 v35, v35\n"
      "v_exp_f32 v36, v36\n"
      "v_exp_f32 v37, v37\n"
      "v_exp_f32 v38, v38\n"
      "v_exp_f32 v39, v39\n"
      "v_exp_f32 v40, v40\n"
      "v_exp_f32 v41, v41\n"
      "v_exp_f32 v42, v42\n"
      "v_exp_f32 v43, v43\n"
      "v_exp_f32 v44, v44\n"
      "v_exp_f32 v45, v45\n"
      "v_exp_f32 v46, v46\n"
      "v_exp_f32 v47, v47\n"
      "s_setprio 2\n"
      "s_waitcnt lgkmcnt(0)\n"
      "ds_read_b32 v209, v215\n"
      "v_mfma_f32_32x32x16_bf16 v[48:63], v[88:91], v[112:115], v[128:143]\n"
      "s_add_u32 s43, s43, 8\n"
      "ds_read_b64_tr_b16 v[104:105], v214 offset:22528\n"
      "v_mfma_f32_32x32x16_bf16 v[64:79], v[88:91], v[120:123], v[144:159]\n"
      "ds_read_b64_tr_b16 v[106:107], v214 offset:22784\n"
      "s_add_u32 m0, s64, 8192\n"
      "v_mfma_f32_32x32x16_bf16 v[48:63], v[92:95], v[116:119], v[48:63]\n"
      "ds_read_b64_tr_b16 v[108:109], v214 offset:23552\n"
      "s_add_u32 s41, s41, 8192\n"
      "v_mfma_f32_32x32x16_bf16 v[64:79], v[92:95], v[124:127], v[64:79]\n"
      "ds_read_b64_tr_b16 v[110:111], v214 offset:23808\n"
      "v_smfmac_f32_32x32x32_bf16 v[160:175], v[96:99], v[16:23], v216\n"
      "s_waitcnt lgkmcnt(4)\n"
      "v_cmp_le_u32 vcc, s43, v209\n"
      "s_cmp_lg_u64 vcc, 0\n"
      "s_cbranch_scc0 LS10_%=\n"
      "LR10_%=:\n"
      "v_smfmac_f32_32x32x32_bf16 v[176:191], v[96:99], v[32:39], v216\n"
      "ds_read_b128 v[80:83], v213 offset:24576\n"
      "v_smfmac_f32_32x32x32_bf16 v[160:175], v[100:103], v[24:31], v216\n"
      "ds_read_b128 v[84:87], v213 offset:25088\n"
      "v_smfmac_f32_32x32x32_bf16 v[176:191], v[100:103], v[40:47], v216\n"
      "buffer_load_dwordx4 v211, s[52:55], s41 offen lds\n"
      "v_smfmac_f32_16x16x64_bf16 v[192:195], v[200:203], v[16:23], v216\n"
      "v_smfmac_f32_16x16x64_bf16 v[196:199], v[200:203], v[32:39], v216\n"
      "buffer_load_dwordx4 v211, s[52:55], s41 offen offset:1024 lds\n"
      "v_smfmac_f32_16x16x64_bf16 v[192:195], v[200:203], v[24:31], v216\n"
      "v_smfmac_f32_16x16x64_bf16 v[196:199], v[200:203], v[40:47], v216\n"
      "s_setprio 0\n"
      "v_exp_f32 v48, v48\n"
      "v_exp_f32 v49, v49\n"
      "v_exp_f32 v50, v50\n"
      "v_exp_f32 v51, v51\n"
      "s_setprio 1\n"
      "v_exp_f32 v52, v52\n"
      "v_exp_f32 v53, v53\n"
      "v_exp_f32 v54, v54\n"
      "v_exp_f32 v55, v55\n"
      "v_exp_f32 v56, v56\n"
      "v_exp_f32 v57, v57\n"
      "v_exp_f32 v58, v58\n"
      "v_exp_f32 v59, v59\n"
      "v_exp_f32 v60, v60\n"
      "v_exp_f32 v61, v61\n"
      "v_exp_f32 v62, v62\n"
      "v_exp_f32 v63, v63\n"
      "v_exp_f32 v64, v64\n"
      "v_exp_f32 v65, v65\n"
      "v_exp_f32 v66, v66\n"
      "v_exp_f32 v67, v67\n"
      "v_exp_f32 v68, v68\n"
      "v_exp_f32 v69, v69\n"
      "v_exp_f32 v70, v70\n"
      "v_exp_f32 v71, v71\n"
      "v_exp_f32 v72, v72\n"
      "v_exp_f32 v73, v73\n"
      "v_exp_f32 v74, v74\n"
      "v_exp_f32 v75, v75\n"
      "v_exp_f32 v76, v76\n"
      "v_exp_f32 v77, v77\n"
      "v_exp_f32 v78, v78\n"
      "v_exp_f32 v79, v79\n"
      "s_setprio 2\n"
      "s_waitcnt lgkmcnt(0)\n"
      "v_mfma_f32_32x32x16_bf16 v[16:31], v[80:83], v[112:115], v[128:143]\n"
      "ds_read_b128 v[88:91], v213 offset:26624\n"
      "v_mfma_f32_32x32x16_bf16 v[32:47], v[80:83], v[120:123], v[144:159]\n"
      "ds_read_b128 v[92:95], v213 offset:27136\n"
      "v_mfma_f32_32x32x16_bf16 v[16:31], v[84:87], v[116:119], v[16:31]\n"
      "v_mfma_f32_32x32x16_bf16 v[32:47], v[84:87], v[124:127], v[32:47]\n"
      "ds_read_b64_tr_b16 v[96:97], v214 offset:24576\n"
      "v_smfmac_f32_32x32x32_bf16 v[160:175], v[104:107], v[48:55], v216\n"
      "v_smfmac_f32_32x32x32_bf16 v[176:191], v[104:107], v[64:71], v216\n"
      "ds_read_b64_tr_b16 v[98:99], v214 offset:24832\n"
      "v_smfmac_f32_32x32x32_bf16 v[160:175], v[108:111], v[56:63], v216\n"
      "ds_read_b64_tr_b16 v[100:101], v214 offset:25600\n"
      "v_smfmac_f32_32x32x32_bf16 v[176:191], v[108:111], v[72:79], v216\n"
      "v_smfmac_f32_16x16x64_bf16 v[192:195], v[200:203], v[48:55], v216\n"
      "ds_read_b64_tr_b16 v[102:103], v214 offset:25856\n"
      "v_smfmac_f32_16x16x64_bf16 v[196:199], v[200:203], v[64:71], v216\n"
      "v_smfmac_f32_16x16x64_bf16 v[192:195], v[200:203], v[56:63], v216\n"
      "v_smfmac_f32_16x16x64_bf16 v[196:199], v[200:203], v[72:79], v216\n"
      "s_setprio 0\n"
      "v_exp_f32 v16, v16\n"
      "v_exp_f32 v17, v17\n"
      "v_exp_f32 v18, v18\n"
      "v_exp_f32 v19, v19\n"
      "s_setprio 1\n"
      "v_exp_f32 v20, v20\n"
      "v_exp_f32 v21, v21\n"
      "v_exp_f32 v22, v22\n"
      "v_exp_f32 v23, v23\n"
      "v_exp_f32 v24, v24\n"
      "v_exp_f32 v25, v25\n"
      "v_exp_f32 v26, v26\n"
      "v_exp_f32 v27, v27\n"
      "v_exp_f32 v28, v28\n"
      "v_exp_f32 v29, v29\n"
      "v_exp_f32 v30, v30\n"
      "v_exp_f32 v31, v31\n"
      "v_exp_f32 v32, v32\n"
      "v_exp_f32 v33, v33\n"
      "v_exp_f32 v34, v34\n"
      "v_exp_f32 v35, v35\n"
      "v_exp_f32 v36, v36\n"
      "v_exp_f32 v37, v37\n"
      "v_exp_f32 v38, v38\n"
      "v_exp_f32 v39, v39\n"
      "v_exp_f32 v40, v40\n"
      "v_exp_f32 v41, v41\n"
      "v_exp_f32 v42, v42\n"
      "v_exp_f32 v43, v43\n"
      "v_exp_f32 v44, v44\n"
      "v_exp_f32 v45, v45\n"
      "v_exp_f32 v46, v46\n"
      "v_exp_f32 v47, v47\n"
      "s_setprio 2\n"
      "s_waitcnt lgkmcnt(0)\n"
      "v_mfma_f32_32x32x16_bf16 v[48:63], v[88:91], v[112:115], v[128:143]\n"
      "ds_read_b128 v[80:83], v213 offset:28672\n"
      "v_mfma_f32_32x32x16_bf16 v[64:79], v[88:91], v[120:123], v[144:159]\n"
      "ds_read_b128 v[84:87], v213 offset:29184\n"
      "v_mfma_f32_32x32x16_bf16 v[48:63], v[92:95], v[116:119], v[48:63]\n"
      "v_mfma_f32_32x32x16_bf16 v[64:79], v[92:95], v[124:127], v[64:79]\n"
      "ds_read_b64_tr_b16 v[104:105], v214 offset:26624\n"
      "v_smfmac_f32_32x32x32_bf16 v[160:175], v[96:99], v[16:23], v216\n"
      "v_smfmac_f32_32x32x32_bf16 v[176:191], v[96:99], v[32:39], v216\n"
      "ds_read_b64_tr_b16 v[106:107], v214 offset:26880\n"
      "v_smfmac_f32_32x32x32_bf16 v[160:175], v[100:103], v[24:31], v216\n"
      "ds_read_b64_tr_b16 v[108:109], v214 offset:27648\n"
      "v_smfmac_f32_32x32x32_bf16 v[176:191], v[100:103], v[40:47], v216\n"
      "v_smfmac_f32_16x16x64_bf16 v[192:195], v[200:203], v[16:23], v216\n"
      "ds_read_b64_tr_b16 v[110:111], v214 offset:27904\n"
      "v_smfmac_f32_16x16x64_bf16 v[196:199], v[200:203], v[32:39], v216\n"
      "v_smfmac_f32_16x16x64_bf16 v[192:195], v[200:203], v[24:31], v216\n"
      "s_waitcnt vmcnt(2)\n"
      "ds_add_u32 v215, v208\n"
      "v_smfmac_f32_16x16x64_bf16 v[196:199], v[200:203], v[40:47], v216\n"
      "s_setprio 0\n"
      "v_exp_f32 v48, v48\n"
      "v_exp_f32 v49, v49\n"
      "v_exp_f32 v50, v50\n"
      "v_exp_f32 v51, v51\n"
      "s_setprio 1\n"
      "v_exp_f32 v52, v52\n"
      "v_exp_f32 v53, v53\n"
      "v_exp_f32 v54, v54\n"
      "v_exp_f32 v55, v55\n"
      "v_exp_f32 v56, v56\n"
      "v_exp_f32 v57, v57\n"
      "v_exp_f32 v58, v58\n"
      "v_exp_f32 v59, v59\n"
      "v_exp_f32 v60, v60\n"
      "v_exp_f32 v61, v61\n"
      "v_exp_f32 v62, v62\n"
      "v_exp_f32 v63, v63\n"
      "v_exp_f32 v64, v64\n"
      "v_exp_f32 v65, v65\n"
      "v_exp_f32 v66, v66\n"
      "v_exp_f32 v67, v67\n"
      "v_exp_f32 v68, v68\n"
      "v_exp_f32 v69, v69\n"
      "v_exp_f32 v70, v70\n"
      "v_exp_f32 v71, v71\n"
      "v_exp_f32 v72, v72\n"
      "v_exp_f32 v73, v73\n"
      "v_exp_f32 v74, v74\n"
      "v_exp_f32 v75, v75\n"
      "v_exp_f32 v76, v76\n"
      "v_exp_f32 v77, v77\n"
      "v_exp_f32 v78, v78\n"
      "v_exp_f32 v79, v79\n"
      "s_setprio 2\n"
      "s_waitcnt lgkmcnt(0)\n"
      "v_mfma_f32_32x32x16_bf16 v[16:31], v[80:83], v[112:115], v[128:143]\n"
      "ds_read_b128 v[88:91], v213 offset:30720\n"
      "v_mfma_f32_32x32x16_bf16 v[32:47], v[80:83], v[120:123], v[144:159]\n"
      "ds_read_b128 v[92:95], v213 offset:31232\n"
      "v_mfma_f32_32x32x16_bf16 v[16:31], v[84:87], v[116:119], v[16:31]\n"
      "v_mfma_f32_32x32x16_bf16 v[32:47], v[84:87], v[124:127], v[32:47]\n"
      "ds_read_b64_tr_b16 v[96:97], v214 offset:28672\n"
      "v_smfmac_f32_32x32x32_bf16 v[160:175], v[104:107], v[48:55], v216\n"
      "v_smfmac_f32_32x32x32_bf16 v[176:191], v[104:107], v[64:71], v216\n"
      "ds_read_b64_tr_b16 v[98:99], v214 offset:28928\n"
      "v_smfmac_f32_32x32x32_bf16 v[160:175], v[108:111], v[56:63], v216\n"
      "ds_read_b64_tr_b16 v[100:101], v214 offset:29696\n"
      "v_smfmac_f32_32x32x32_bf16 v[176:191], v[108:111], v[72:79], v216\n"
      "v_smfmac_f32_16x16x64_bf16 v[192:195], v[200:203], v[48:55], v216\n"
      "ds_read_b64_tr_b16 v[102:103], v214 offset:29952\n"
      "v_smfmac_f32_16x16x64_bf16 v[196:199], v[200:203], v[64:71], v216\n"
      "v_smfmac_f32_16x16x64_bf16 v[192:195], v[200:203], v[56:63], v216\n"
      "v_smfmac_f32_16x16x64_bf16 v[196:199], v[200:203], v[72:79], v216\n"
      "s_setprio 0\n"
      "v_exp_f32 v16, v16\n"
      "v_exp_f32 v17, v17\n"
      "v_exp_f32 v18, v18\n"
      "v_exp_f32 v19, v19\n"
      "s_setprio 1\n"
      "v_exp_f32 v20, v20\n"
      "v_exp_f32 v21, v21\n"
      "v_exp_f32 v22, v22\n"
      "v_exp_f32 v23, v23\n"
      "v_exp_f32 v24, v24\n"
      "v_exp_f32 v25, v25\n"
      "v_exp_f32 v26, v26\n"
      "v_exp_f32 v27, v27\n"
      "v_exp_f32 v28, v28\n"
      "v_exp_f32 v29, v29\n"
      "v_exp_f32 v30, v30\n"
      "v_exp_f32 v31, v31\n"
      "v_exp_f32 v32, v32\n"
      "v_exp_f32 v33, v33\n"
      "v_exp_f32 v34, v34\n"
      "v_exp_f32 v35, v35\n"
      "v_exp_f32 v36, v36\n"
      "v_exp_f32 v37, v37\n"
      "v_exp_f32 v38, v38\n"
      "v_exp_f32 v39, v39\n"
      "v_exp_f32 v40, v40\n"
      "v_exp_f32 v41, v41\n"
      "v_exp_f32 v42, v42\n"
      "v_exp_f32 v43, v43\n"
      "v_exp_f32 v44, v44\n"
      "v_exp_f32 v45, v45\n"
      "v_exp_f32 v46, v46\n"
      "v_exp_f32 v47, v47\n"
      "s_setprio 2\n"
      "s_waitcnt lgkmcnt(0)\n"
      "ds_read_b32 v209, v215\n"
      "v_mfma_f32_32x32x16_bf16 v[48:63], v[88:91], v[112:115], v[128:143]\n"
      "s_add_u32 s43, s43, 8\n"
      "ds_read_b64_tr_b16 v[104:105], v214 offset:30720\n"
      "v_mfma_f32_32x32x16_bf16 v[64:79], v[88:91], v[120:123], v[144:159]\n"
      "ds_read_b64_tr_b16 v[106:107], v214 offset:30976\n"
      "s_add_u32 m0, s64, 16384\n"
      "v_mfma_f32_32x32x16_bf16 v[48:63], v[92:95], v[116:119], v[48:63]\n"
      "ds_read_b64_tr_b16 v[108:109], v214 offset:31744\n"
      "s_add_u32 s41, s41, 8192\n"
      "v_mfma_f32_32x32x16_bf16 v[64:79], v[92:95], v[124:127], v[64:79]\n"
      "ds_read_b64_tr_b16 v[110:111], v214 offset:32000\n"
      "v_smfmac_f32_32x32x32_bf16 v[160:175], v[96:99], v[16:23], v216\n"
      "s_waitcnt lgkmcnt(4)\n"
      "v_cmp_le_u32 vcc, s43, v209\n"
      "s_cmp_lg_u64 vcc, 0\n"
      "s_cbranch_scc0 LS14_%=\n"
      "LR14_%=:\n"
      "v_smfmac_f32_32x32x32_bf16 v[176:191], v[96:99], v[32:39], v216\n"
      "ds_read_b128 v[80:83], v213 offset:0\n"
      "v_smfmac_f32_32x32x32_bf16 v[160:175], v[100:103], v[24:31], v216\n"
      "ds_read_b128 v[84:87], v213 offset:512\n"
      "v_smfmac_f32_32x32x32_bf16 v[176:191], v[100:103], v[40:47], v216\n"
      "buffer_load_dwordx4 v211, s[52:55], s41 offen lds\n"
      "v_smfmac_f32_16x16x64_bf16 v[192:195], v[200:203], v[16:23], v216\n"
      "v_smfmac_f32_16x16x64_bf16 v[196:199], v[200:203], v[32:39], v216\n"
      "buffer_load_dwordx4 v211, s[52:55], s41 offen offset:1024 lds\n"
      "v_smfmac_f32_16x16x64_bf16 v[192:195], v[200:203], v[24:31], v216\n"
      "v_smfmac_f32_16x16x64_bf16 v[196:199], v[200:203], v[40:47], v216\n"
      "s_sub_u32 s42, s42, 1\n"
      "s_cmp_eq_u32 s42, 0\n"
      "s_setprio 0\n"
      "s_cbranch_scc1 LEXIT_%=\n"
      "v_exp_f32 v48, v48\n"
      "v_exp_f32 v49, v49\n"
      "v_exp_f32 v50, v50\n"
      "v_exp_f32 v51, v51\n"
      "s_setprio 1\n"
      "v_exp_f32 v52, v52\n"
      "v_exp_f32 v53, v53\n"
      "v_exp_f32 v54, v54\n"
      "v_exp_f32 v55, v55\n"
      "v_exp_f32 v56, v56\n"
      "v_exp_f32 v57, v57\n"
      "v_exp_f32 v58, v58\n"
      "v_exp_f32 v59, v59\n"
      "v_exp_f32 v60, v60\n"
      "v_exp_f32 v61, v61\n"
      "v_exp_f32 v62, v62\n"
      "v_exp_f32 v63, v63\n"
      "v_exp_f32 v64, v64\n"
      "v_exp_f32 v65, v65\n"
      "v_exp_f32 v66, v66\n"
      "v_exp_f32 v67, v67\n"
      "v_exp_f32 v68, v68\n"
      "v_exp_f32 v69, v69\n"
      "v_exp_f32 v70, v70\n"
      "v_exp_f32 v71, v71\n"
      "v_exp_f32 v72, v72\n"
      "v_exp_f32 v73, v73\n"
      "v_exp_f32 v74, v74\n"
      "v_exp_f32 v75, v75\n"
      "v_exp_f32 v76, v76\n"
      "v_exp_f32 v77, v77\n"
      "v_exp_f32 v78, v78\n"
      "v_exp_f32 v79, v79\n"
      "s_setprio 2\n"
      "s_waitcnt lgkmcnt(0)\n"
      "v_mfma_f32_32x32x16_bf16 v[16:31], v[80:83], v[112:115], v[128:143]\n"
      "ds_read_b128 v[88:91], v213 offset:2048\n"
      "v_mfma_f32_32x32x16_bf16 v[32:47], v[80:83], v[120:123], v[144:159]\n"
      "ds_read_b128 v[92:95], v213 offset:2560\n"
      "v_mfma_f32_32x32x16_bf16 v[16:31], v[84:87], v[116:119], v[16:31]\n"
      "v_mfma_f32_32x32x16_bf16 v[32:47], v[84:87], v[124:127], v[32:47]\n"
      "ds_read_b64_tr_b16 v[96:97], v214 offset:0\n"
      "v_smfmac_f32_32x32x32_bf16 v[160:175], v[104:107], v[48:55], v216\n"
      "v_smfmac_f32_32x32x32_bf16 v[176:191], v[104:107], v[64:71], v216\n"
      "ds_read_b64_tr_b16 v[98:99], v214 offset:256\n"
      "v_smfmac_f32_32x32x32_bf16 v[160:175], v[108:111], v[56:63], v216\n"
      "ds_read_b64_tr_b16 v[100:101], v214 offset:1024\n"
      "v_smfmac_f32_32x32x32_bf16 v[176:191], v[108:111], v[72:79], v216\n"
      "v_smfmac_f32_16x16x64_bf16 v[192:195], v[200:203], v[48:55], v216\n"
      "ds_read_b64_tr_b16 v[102:103], v214 offset:1280\n"
      "v_smfmac_f32_16x16x64_bf16 v[196:199], v[200:203], v[64:71], v216\n"
      "v_smfmac_f32_16x16x64_bf16 v[192:195], v[200:203], v[56:63], v216\n"
      "v_smfmac_f32_16x16x64_bf16 v[196:199], v[200:203], v[72:79], v216\n"
      "s_setprio 0\n"
      "v_exp_f32 v16, v16\n"
      "v_exp_f32 v17, v17\n"
      "v_exp_f32 v18, v18\n"
      "v_exp_f32 v19, v19\n"
      "s_setprio 1\n"
      "v_exp_f32 v20, v20\n"
      "v_exp_f32 v21, v21\n"
      "v_exp_f32 v22, v22\n"
      "v_exp_f32 v23, v23\n"
      "v_exp_f32 v24, v24\n"
      "v_exp_f32 v25, v25\n"
      "v_exp_f32 v26, v26\n"
      "v_exp_f32 v27, v27\n"
      "v_exp_f32 v28, v28\n"
      "v_exp_f32 v29, v29\n"
      "v_exp_f32 v30, v30\n"
      "v_exp_f32 v31, v31\n"
      "v_exp_f32 v32, v32\n"
      "v_exp_f32 v33, v33\n"
      "v_exp_f32 v34, v34\n"
      "v_exp_f32 v35, v35\n"
      "v_exp_f32 v36, v36\n"
      "v_exp_f32 v37, v37\n"
      "v_exp_f32 v38, v38\n"
      "v_exp_f32 v39, v39\n"
      "v_exp_f32 v40, v40\n"
      "v_exp_f32 v41, v41\n"
      "v_exp_f32 v42, v42\n"
      "v_exp_f32 v43, v43\n"
      "v_exp_f32 v44, v44\n"
      "v_exp_f32 v45, v45\n"
      "v_exp_f32 v46, v46\n"
      "v_exp_f32 v47, v47\n"
      "s_setprio 2\n"
      "s_waitcnt lgkmcnt(0)\n"
      "v_mfma_f32_32x32x16_bf16 v[48:63], v[88:91], v[112:115], v[128:143]\n"
      "ds_read_b128 v[80:83], v213 offset:4096\n"
      "v_mfma_f32_32x32x16_bf16 v[64:79], v[88:91], v[120:123], v[144:159]\n"
      "ds_read_b128 v[84:87], v213 offset:4608\n"
      "v_mfma_f32_32x32x16_bf16 v[48:63], v[92:95], v[116:119], v[48:63]\n"
      "v_mfma_f32_32x32x16_bf16 v[64:79], v[92:95], v[124:127], v[64:79]\n"
      "ds_read_b64_tr_b16 v[104:105], v214 offset:2048\n"
      "v_smfmac_f32_32x32x32_bf16 v[160:175], v[96:99], v[16:23], v216\n"
      "v_smfmac_f32_32x32x32_bf16 v[176:191], v[96:99], v[32:39], v216\n"
      "ds_read_b64_tr_b16 v[106:107], v214 offset:2304\n"
      "v_smfmac_f32_32x32x32_bf16 v[160:175], v[100:103], v[24:31], v216\n"
      "ds_read_b64_tr_b16 v[108:109], v214 offset:3072\n"
      "v_smfmac_f32_32x32x32_bf16 v[176:191], v[100:103], v[40:47], v216\n"
      "v_smfmac_f32_16x16x64_bf16 v[192:195], v[200:203], v[16:23], v216\n"
      "ds_read_b64_tr_b16 v[110:111], v214 offset:3328\n"
      "v_smfmac_f32_16x16x64_bf16 v[196:199], v[200:203], v[32:39], v216\n"
      "v_smfmac_f32_16x16x64_bf16 v[192:195], v[200:203], v[24:31], v216\n"
      "s_waitcnt vmcnt(2)\n"
      "ds_add_u32 v215, v208\n"
      "v_smfmac_f32_16x16x64_bf16 v[196:199], v[200:203], v[40:47], v216\n"
      "s_setprio 0\n"
      "v_exp_f32 v48, v48\n"
      "v_exp_f32 v49, v49\n"
      "v_exp_f32 v50, v50\n"
      "v_exp_f32 v51, v51\n"
      "s_setprio 1\n"
      "v_exp_f32 v52, v52\n"
      "v_exp_f32 v53, v53\n"
      "v_exp_f32 v54, v54\n"
      "v_exp_f32 v55, v55\n"
      "v_exp_f32 v56, v56\n"
      "v_exp_f32 v57, v57\n"
      "v_exp_f32 v58, v58\n"
      "v_exp_f32 v59, v59\n"
      "v_exp_f32 v60, v60\n"
      "v_exp_f32 v61, v61\n"
      "v_exp_f32 v62, v62\n"
      "v_exp_f32 v63, v63\n"
      "v_exp_f32 v64, v64\n"
      "v_exp_f32 v65, v65\n"
      "v_exp_f32 v66, v66\n"
      "v_exp_f32 v67, v67\n"
      "v_exp_f32 v68, v68\n"
      "v_exp_f32 v69, v69\n"
      "v_exp_f32 v70, v70\n"
      "v_exp_f32 v71, v71\n"
      "v_exp_f32 v72, v72\n"
      "v_exp_f32 v73, v73\n"
      "v_exp_f32 v74, v74\n"
      "v_exp_f32 v75, v75\n"
      "v_exp_f32 v76, v76\n"
      "v_exp_f32 v77, v77\n"
      "v_exp_f32 v78, v78\n"
      "v_exp_f32 v79, v79\n"
      "s_setprio 2\n"
      "s_waitcnt lgkmcnt(0)\n"
      "v_mfma_f32_32x32x16_bf16 v[16:31], v[80:83], v[112:115], v[128:143]\n"
      "ds_read_b128 v[88:91], v213 offset:6144\n"
      "v_mfma_f32_32x32x16_bf16 v[32:47], v[80:83], v[120:123], v[144:159]\n"
      "ds_read_b128 v[92:95], v213 offset:6656\n"
      "v_mfma_f32_32x32x16_bf16 v[16:31], v[84:87], v[116:119], v[16:31]\n"
      "v_mfma_f32_32x32x16_bf16 v[32:47], v[84:87], v[124:127], v[32:47]\n"
      "ds_read_b64_tr_b16 v[96:97], v214 offset:4096\n"
      "v_smfmac_f32_32x32x32_bf16 v[160:175], v[104:107], v[48:55], v216\n"
      "v_smfmac_f32_32x32x32_bf16 v[176:191], v[104:107], v[64:71], v216\n"
      "ds_read_b64_tr_b16 v[98:99], v214 offset:4352\n"
      "v_smfmac_f32_32x32x32_bf16 v[160:175], v[108:111], v[56:63], v216\n"
      "ds_read_b64_tr_b16 v[100:101], v214 offset:5120\n"
      "v_smfmac_f32_32x32x32_bf16 v[176:191], v[108:111], v[72:79], v216\n"
      "v_smfmac_f32_16x16x64_bf16 v[192:195], v[200:203], v[48:55], v216\n"
      "ds_read_b64_tr_b16 v[102:103], v214 offset:5376\n"
      "v_smfmac_f32_16x16x64_bf16 v[196:199], v[200:203], v[64:71], v216\n"
      "v_smfmac_f32_16x16x64_bf16 v[192:195], v[200:203], v[56:63], v216\n"
      "v_smfmac_f32_16x16x64_bf16 v[196:199], v[200:203], v[72:79], v216\n"
      "s_setprio 0\n"
      "v_exp_f32 v16, v16\n"
      "v_exp_f32 v17, v17\n"
      "v_exp_f32 v18, v18\n"
      "v_exp_f32 v19, v19\n"
      "s_setprio 1\n"
      "v_exp_f32 v20, v20\n"
      "v_exp_f32 v21, v21\n"
      "v_exp_f32 v22, v22\n"
      "v_exp_f32 v23, v23\n"
      "v_exp_f32 v24, v24\n"
      "v_exp_f32 v25, v25\n"
      "v_exp_f32 v26, v26\n"
      "v_exp_f32 v27, v27\n"
      "v_exp_f32 v28, v28\n"
      "v_exp_f32 v29, v29\n"
      "v_exp_f32 v30, v30\n"
      "v_exp_f32 v31, v31\n"
      "v_exp_f32 v32, v32\n"
      "v_exp_f32 v33, v33\n"
      "v_exp_f32 v34, v34\n"
      "v_exp_f32 v35, v35\n"
      "v_exp_f32 v36, v36\n"
      "v_exp_f32 v37, v37\n"
      "v_exp_f32 v38, v38\n"
      "v_exp_f32 v39, v39\n"
      "v_exp_f32 v40, v40\n"
      "v_exp_f32 v41, v41\n"
      "v_exp_f32 v42, v42\n"
      "v_exp_f32 v43, v43\n"
      "v_exp_f32 v44, v44\n"
      "v_exp_f32 v45, v45\n"
      "v_exp_f32 v46, v46\n"
      "v_exp_f32 v47, v47\n"
      "s_setprio 2\n"
      "s_waitcnt lgkmcnt(0)\n"
      "ds_read_b32 v209, v215\n"
      "v_mfma_f32_32x32x16_bf16 v[48:63], v[88:91], v[112:115], v[128:143]\n"
      "s_add_u32 s43, s43, 8\n"
      "ds_read_b64_tr_b16 v[104:105], v214 offset:6144\n"
      "v_mfma_f32_32x32x16_bf16 v[64:79], v[88:91], v[120:123], v[144:159]\n"
      "ds_read_b64_tr_b16 v[106:107], v214 offset:6400\n"
      "s_add_u32 m0, s64, 24576\n"
      "v_mfma_f32_32x32x16_bf16 v[48:63], v[92:95], v[116:119], v[48:63]\n"
      "ds_read_b64_tr_b16 v[108:109], v214 offset:7168\n"
      "s_add_u32 s41, s41, 8192\n"
      "v_mfma_f32_32x32x16_bf16 v[64:79], v[92:95], v[124:127], v[64:79]\n"
      "ds_read_b64_tr_b16 v[110:111], v214 offset:7424\n"
      "v_smfmac_f32_32x32x32_bf16 v[160:175], v[96:99], v[16:23], v216\n"
      "s_waitcnt lgkmcnt(4)\n"
      "v_cmp_le_u32 vcc, s43, v209\n"
      "s_cmp_lg_u64 vcc, 0\n"
      "s_cbranch_scc0 LS18_%=\n"
      "LR18_%=:\n"
      "v_smfmac_f32_32x32x32_bf16 v[176:191], v[96:99], v[32:39], v216\n"
      "ds_read_b128 v[80:83], v213 offset:8192\n"
      "v_smfmac_f32_32x32x32_bf16 v[160:175], v[100:103], v[24:31], v216\n"
      "ds_read_b128 v[84:87], v213 offset:8704\n"
      "v_smfmac_f32_32x32x32_bf16 v[176:191], v[100:103], v[40:47], v216\n"
      "buffer_load_dwordx4 v211, s[52:55], s41 offen lds\n"
      "v_smfmac_f32_16x16x64_bf16 v[192:195], v[200:203], v[16:23], v216\n"
      "v_smfmac_f32_16x16x64_bf16 v[196:199], v[200:203], v[32:39], v216\n"
      "buffer_load_dwordx4 v211, s[52:55], s41 offen offset:1024 lds\n"
      "v_smfmac_f32_16x16x64_bf16 v[192:195], v[200:203], v[24:31], v216\n"
      "v_smfmac_f32_16x16x64_bf16 v[196:199], v[200:203], v[40:47], v216\n"
      "s_setprio 0\n"
      "v_exp_f32 v48, v48\n"
      "v_exp_f32 v49, v49\n"
      "v_exp_f32 v50, v50\n"
      "v_exp_f32 v51, v51\n"
      "s_setprio 1\n"
      "v_exp_f32 v52, v52\n"
      "v_exp_f32 v53, v53\n"
      "v_exp_f32 v54, v54\n"
      "v_exp_f32 v55, v55\n"
      "v_exp_f32 v56, v56\n"
      "v_exp_f32 v57, v57\n"
      "v_exp_f32 v58, v58\n"
      "v_exp_f32 v59, v59\n"
      "v_exp_f32 v60, v60\n"
      "v_exp_f32 v61, v61\n"
      "v_exp_f32 v62, v62\n"
      "v_exp_f32 v63, v63\n"
      "v_exp_f32 v64, v64\n"
      "v_exp_f32 v65, v65\n"
      "v_exp_f32 v66, v66\n"
      "v_exp_f32 v67, v67\n"
      "v_exp_f32 v68, v68\n"
      "v_exp_f32 v69, v69\n"
      "v_exp_f32 v70, v70\n"
      "v_exp_f32 v71, v71\n"
      "v_exp_f32 v72, v72\n"
      "v_exp_f32 v73, v73\n"
      "v_exp_f32 v74, v74\n"
      "v_exp_f32 v75, v75\n"
      "v_exp_f32 v76, v76\n"
      "v_exp_f32 v77, v77\n"
      "v_exp_f32 v78, v78\n"
      "v_exp_f32 v79, v79\n"
      "s_setprio 2\n"
      "s_waitcnt lgkmcnt(0)\n"
      "v_mfma_f32_32x32x16_bf16 v[16:31], v[80:83], v[112:115], v[128:143]\n"
      "ds_read_b128 v[88:91], v213 offset:10240\n"
      "v_mfma_f32_32x32x16_bf16 v[32:47], v[80:83], v[120:123], v[144:159]\n"
      "ds_read_b128 v[92:95], v213 offset:10752\n"
      "v_mfma_f32_32x32x16_bf16 v[16:31], v[84:87], v[116:119], v[16:31]\n"
      "v_mfma_f32_32x32x16_bf16 v[32:47], v[84:87], v[124:127], v[32:47]\n"
      "ds_read_b64_tr_b16 v[96:97], v214 offset:8192\n"
      "v_smfmac_f32_32x32x32_bf16 v[160:175], v[104:107], v[48:55], v216\n"
      "v_smfmac_f32_32x32x32_bf16 v[176:191], v[104:107], v[64:71], v216\n"
      "ds_read_b64_tr_b16 v[98:99], v214 offset:8448\n"
      "v_smfmac_f32_32x32x32_bf16 v[160:175], v[108:111], v[56:63], v216\n"
      "ds_read_b64_tr_b16 v[100:101], v214 offset:9216\n"
      "v_smfmac_f32_32x32x32_bf16 v[176:191], v[108:111], v[72:79], v216\n"
      "v_smfmac_f32_16x16x64_bf16 v[192:195], v[200:203], v[48:55], v216\n"
      "ds_read_b64_tr_b16 v[102:103], v214 offset:9472\n"
      "v_smfmac_f32_16x16x64_bf16 v[196:199], v[200:203], v[64:71], v216\n"
      "v_smfmac_f32_16x16x64_bf16 v[192:195], v[200:203], v[56:63], v216\n"
      "v_smfmac_f32_16x16x64_bf16 v[196:199], v[200:203], v[72:79], v216\n"
      "s_setprio 0\n"
      "s_branch LL_%=\n"
      "LEXIT_%=:\n"
      "v_exp_f32 v48, v48\n"
      "v_exp_f32 v49, v49\n"
      "v_exp_f32 v50, v50\n"
      "v_exp_f32 v51, v51\n"
      "s_setprio 1\n"
      "v_exp_f32 v52, v52\n"
      "v_exp_f32 v53, v53\n"
      "v_exp_f32 v54, v54\n"
      "v_exp_f32 v55, v55\n"
      "v_exp_f32 v56, v56\n"
      "v_exp_f32 v57, v57\n"
      "v_exp_f32 v58, v58\n"
      "v_exp_f32 v59, v59\n"
      "v_exp_f32 v60, v60\n"
      "v_exp_f32 v61, v61\n"
      "v_exp_f32 v62, v62\n"
      "v_exp_f32 v63, v63\n"
      "s_mov_b32 s67, 0x53800000\n"
      "s_waitcnt lgkmcnt(0)\n"
      "v_smfmac_f32_32x32x32_bf16 v[160:175], v[104:107], v[48:55], v216\n"
      "v_exp_f32 v64, v64\n"
      "v_exp_f32 v65, v65\n"
      "v_exp_f32 v66, v66\n"
      "v_smfmac_f32_16x16x64_bf16 v[192:195], v[200:203], v[48:55], v216\n"
      "v_exp_f32 v67, v67\n"
      "v_exp_f32 v68, v68\n"
      "v_smfmac_f32_32x32x32_bf16 v[160:175], v[108:111], v[56:63], v216\n"
      "v_exp_f32 v69, v69\n"
      "v_exp_f32 v70, v70\n"
      "v_exp_f32 v71, v71\n"
      "v_smfmac_f32_16x16x64_bf16 v[192:195], v[200:203], v[56:63], v216\n"
      "v_exp_f32 v72, v72\n"
      "v_exp_f32 v73, v73\n"
      "v_exp_f32 v74, v74\n"
      "v_exp_f32 v75, v75\n"
      "v_exp_f32 v76, v76\n"
      "v_exp_f32 v77, v77\n"
      "v_exp_f32 v78, v78\n"
      "v_exp_f32 v79, v79\n"
      "v_mov_b32 v36, 0\n"
      "v_mov_b32 v37, 0\n"
      "v_mov_b32 v38, 0\n"
      "v_mov_b32 v39, 0\n"
      "v_smfmac_f32_32x32x32_bf16 v[176:191], v[104:107], v[64:71], v216\n"
      "v_permlane16_swap_b32 v192, v193\n"
      "v_mov_b32 v204, v192\n"
      "s_nop 1\n"
      "v_permlane32_swap_b32 v192, v204\n"
      "v_rcp_f32 v204, v192\n"
      "v_cmp_gt_f32 s[70:71], s67, v192\n"
      "v_mul_f32 v160, v204, v160\n"
      "v_mul_f32 v161, v204, v161\n"
      "v_smfmac_f32_16x16x64_bf16 v[196:199], v[200:203], v[64:71], v216\n"
      "v_mul_f32 v162, v204, v162\n"
      "v_mul_f32 v163, v204, v163\n"
      "v_mul_f32 v164, v204, v164\n"
      "v_mul_f32 v165, v204, v165\n"
      "v_smfmac_f32_32x32x32_bf16 v[176:191], v[108:111], v[72:79], v216\n"
      "v_mul_f32 v166, v204, v166\n"
      "v_mul_f32 v167, v204, v167\n"
      "v_mul_f32 v168, v204, v168\n"
      "v_mul_f32 v169, v204, v169\n"
      "v_mul_f32 v170, v204, v170\n"
      "v_mul_f32 v171, v204, v171\n"
      "v_smfmac_f32_16x16x64_bf16 v[196:199], v[200:203], v[72:79], v216\n"
      "v_mul_f32 v172, v204, v172\n"
      "v_mul_f32 v173, v204, v173\n"
      "v_mul_f32 v174, v204, v174\n"
      "v_mul_f32 v175, v204, v175\n"
      "v_cvt_pk_bf16_f32 v16, v160, v161\n"
      "v_cvt_pk_bf16_f32 v17, v162, v163\n"
      "v_cvt_pk_bf16_f32 v18, v164, v165\n"
      "v_cvt_pk_bf16_f32 v19, v166, v167\n"
      "v_cvt_pk_bf16_f32 v20, v168, v169\n"
      "v_cvt_pk_bf16_f32 v21, v170, v171\n"
      "v_cvt_pk_bf16_f32 v22, v172, v173\n"
      "v_cvt_pk_bf16_f32 v23, v174, v175\n"
      "s_nop 0\n"
      "v_permlane32_swap_b32 v16, v18\n"
      "v_permlane32_swap_b32 v17, v19\n"
      "v_permlane32_swap_b32 v20, v22\n"
      "v_permlane32_swap_b32 v21, v23\n"
      "s_nop 0\n"
      "v_permlane16_swap_b32 v16, v20\n"
      "v_permlane16_swap_b32 v17, v21\n"
      "v_permlane16_swap_b32 v18, v22\n"
      "v_permlane16_swap_b32 v19, v23\n"
      "buffer_store_dwordx4 v[16:19], v219, s[60:63], s65 offen offset:0 nt\n"
      "buffer_store_dwordx4 v[20:23], v219, s[60:63], s65 offen offset:1024 nt\n"
      "v_permlane16_swap_b32 v196, v197\n"
      "v_mov_b32 v206, v196\n"
      "s_nop 1\n"
      "v_permlane32_swap_b32 v196, v206\n"
      "v_rcp_f32 v206, v196\n"
      "v_cmp_gt_f32 s[72:73], s67, v196\n"
      "v_pk_mul_f32 v[176:177], v[206:207], v[176:177] op_sel_hi:[0,1]\n"
      "v_pk_mul_f32 v[178:179], v[206:207], v[178:179] op_sel_hi:[0,1]\n"
      "v_pk_mul_f32 v[180:181], v[206:207], v[180:181] op_sel_hi:[0,1]\n"
      "v_pk_mul_f32 v[182:183], v[206:207], v[182:183] op_sel_hi:[0,1]\n"
      "v_pk_mul_f32 v[184:185], v[206:207], v[184:185] op_sel_hi:[0,1]\n"
      "v_pk_mul_f32 v[186:187], v[206:207], v[186:187] op_sel_hi:[0,1]\n"
      "v_pk_mul_f32 v[188:189], v[206:207], v[188:189] op_sel_hi:[0,1]\n"
      "v_pk_mul_f32 v[190:191], v[206:207], v[190:191] op_sel_hi:[0,1]\n"
      "v_cvt_pk_bf16_f32 v24, v176, v177\n"
      "v_cvt_pk_bf16_f32 v25, v178, v179\n"
      "v_cvt_pk_bf16_f32 v26, v180, v181\n"
      "v_cvt_pk_bf16_f32 v27, v182, v183\n"
      "v_cvt_pk_bf16_f32 v28, v184, v185\n"
      "v_cvt_pk_bf16_f32 v29, v186, v187\n"
      "v_cvt_pk_bf16_f32 v30, v188, v189\n"
      "v_cvt_pk_bf16_f32 v31, v190, v191\n"
      "s_nop 0\n"
      "v_permlane32_swap_b32 v24, v26\n"
      "v_permlane32_swap_b32 v25, v27\n"
      "v_permlane32_swap_b32 v28, v30\n"
      "v_permlane32_swap_b32 v29, v31\n"
      "s_nop 0\n"
      "v_permlane16_swap_b32 v24, v28\n"
      "v_permlane16_swap_b32 v25, v29\n"
      "v_permlane16_swap_b32 v26, v30\n"
      "v_permlane16_swap_b32 v27, v31\n"
      "buffer_store_dwordx4 v[24:27], v219, s[60:63], s65 offen offset:2048 nt\n"
      "buffer_store_dwordx4 v[28:31], v219, s[60:63], s65 offen offset:3072 nt\n"
      "v_mfma_f32_16x16x32_bf16 v[32:35], v[36:39], v[16:19], 0\n"
      "v_mfma_f32_16x16x32_bf16 v[32:35], v[36:39], v[20:23], v[32:35]\n"
      "v_mfma_f32_16x16x32_bf16 v[32:35], v[36:39], v[24:27], v[32:35]\n"
      "v_mfma_f32_16x16x32_bf16 v[32:35], v[36:39], v[28:31], v[32:35]\n"
      "s_and_b64 s[70:71], s[70:71], s[72:73]\n"
      "s_mov_b32 m0, s40\n"
      "v_mov_b32 %[tido], %[tid]\n"
      "s_setprio 0\n"
      "s_nop 6\n"
      "v_cmp_o_f32 vcc, v32, v32\n"
      "s_and_b64 s[70:71], s[70:71], vcc\n"
      "s_cmp_eq_u64 s[70:71], exec\n"
      "s_cselect_b32 %[bad], 0, 1\n"
      "s_waitcnt vmcnt(4) lgkmcnt(0)\n"
      "s_branch LEND_%=\n"
      "LSH0_%=:\n"
      "s_sleep 1\n"
      "ds_read_b32 v209, v215 offset:1024\n"
      "s_waitcnt lgkmcnt(0)\n"
      "v_cmp_le_u32 vcc, 4, v209\n"
      "s_cmp_lg_u64 vcc, 0\n"
      "s_cbranch_scc0 LSH0_%=\n"
      "s_branch LRH0_%=\n"
      "LSH1_%=:\n"
      "s_sleep 1\n"
      "ds_read_b32 v209, v215 offset:256\n"
      "s_waitcnt lgkmcnt(0)\n"
      "v_cmp_le_u32 vcc, 4, v209\n"
      "s_cmp_lg_u64 vcc, 0\n"
      "s_cbranch_scc0 LSH1_%=\n"
      "s_branch LRH1_%=\n"
      "LSH3_%=:\n"
      "s_sleep 1\n"
      "ds_read_b32 v209, v215 offset:768\n"
      "s_waitcnt lgkmcnt(0)\n"
      "v_cmp_le_u32 vcc, 4, v209\n"
      "s_cmp_lg_u64 vcc, 0\n"
      "s_cbranch_scc0 LSH3_%=\n"
      "s_branch LRH3_%=\n"
      "LS6_%=:\n"
      "s_sleep 1\n"
      "ds_read_b32 v209, v215\n"
      "s_waitcnt lgkmcnt(0)\n"
      "v_cmp_le_u32 vcc, s43, v209\n"
      "s_cmp_lg_u64 vcc, 0\n"
      "s_cbranch_scc0 LS6_%=\n"
      "s_branch LR6_%=\n"
      "LS10_%=:\n"
      "s_sleep 1\n"
      "ds_read_b32 v209, v215\n"
      "s_waitcnt lgkmcnt(0)\n"
      "v_cmp_le_u32 vcc, s43, v209\n"
      "s_cmp_lg_u64 vcc, 0\n"
      "s_cbranch_scc0 LS10_%=\n"
      "s_branch LR10_%=\n"
      "LS14_%=:\n"
      "s_sleep 1\n"
      "ds_read_b32 v209, v215\n"
      "s_waitcnt lgkmcnt(0)\n"
      "v_cmp_le_u32 vcc, s43, v209\n"
      "s_cmp_lg_u64 vcc, 0\n"
      "s_cbranch_scc0 LS14_%=\n"
      "s_branch LR14_%=\n"
      "LS18_%=:\n"
      "s_sleep 1\n"
      "ds_read_b32 v209, v215\n"
      "s_waitcnt lgkmcnt(0)\n"
      "v_cmp_le_u32 vcc, s43, v209\n"
      "s_cmp_lg_u64 vcc, 0\n"
      "s_cbranch_scc0 LS18_%=\n"
      "s_branch LR18_%=\n"
      "LEND_%=:\n"
      : [tido] "=&v"(tid), [bad] "=&s"(bad)
      : [ka] "s"(__builtin_amdgcn_kernarg_segment_ptr()), [bid] "s"(blockIdx.x), [lds0] "s"(lds0), [tid] "v"(tid0)
      : "v16", "v17", "v18", "v19", "v20", "v21", "v22", "v23", "v24", "v25", "v26", "v27", "v28", "v29", "v30", "v31", "v32", "v33", "v34", "v35", "v36", "v37", "v38", "v39", "v40", "v41", "v42", "v43", "v44", "v45", "v46", "v47", "v48", "v49", "v50", "v51", "v52", "v53", "v54", "v55", "v56", "v57", "v58", "v59", "v60", "v61", "v62", "v63", "v64", "v65", "v66", "v67", "v68", "v69", "v70", "v71", "v72", "v73", "v74", "v75", "v76", "v77", "v78", "v79", "v80", "v81", "v82", "v83", "v84", "v85", "v86", "v87", "v88", "v89", "v90", "v91", "v92", "v93", "v94", "v95", "v96", "v97", "v98", "v99", "v100", "v101", "v102", "v103", "v104", "v105", "v106", "v107", "v108", "v109", "v110", "v111", "v112", "v113", "v114", "v115", "v116", "v117", "v118", "v119", "v120", "v121", "v122", "v123", "v124", "v125", "v126", "v127", "v128", "v129", "v130", "v131", "v132", "v133", "v134", "v135", "v136", "v137", "v138", "v139", "v140", "v141", "v142", "v143", "v144", "v145", "v146", "v147", "v148", "v149", "v150", "v151", "v152", "v153", "v154", "v155", "v156", "v157", "v158", "v159", "v160", "v161", "v162", "v163", "v164", "v165", "v166", "v167", "v168", "v169", "v170", "v171", "v172", "v173", "v174", "v175", "v176", "v177", "v178", "v179", "v180", "v181", "v182", "v183", "v184", "v185", "v186", "v187", "v188", "v189", "v190", "v191", "v192", "v193", "v194", "v195", "v196", "v197", "v198", "v199", "v200", "v201", "v202", "v203", "v204", "v205", "v206", "v207", "v208", "v209", "v210", "v211", "v212", "v213", "v214", "v215", "v216", "v217", "v218", "v219", "v220", "v221", "v222", "v223", "v224", "v225", "v226", "v227", "v228", "v229", "v230", "v231", "v232", "v233", "v234", "v235", "v236", "v237", "v238", "v239", "v240", "v241", "v242", "v243", "v244", "v245", "v246", "v247", "v248", "v249", "v250", "v251", "v252", "v253", "s30", "s31", "s36", "s37", "s38", "s39", "s40", "s41", "s42", "s43", "s44", "s45", "s46", "s47", "s48", "s49", "s50", "s51", "s52", "s53", "s54", "s55", "s56", "s57", "s58", "s59",
        "s60", "s61", "s62", "s63", "s64", "s65", "s66", "s67", "s68", "s69", "s70", "s71", "s72", "s73", "scc", "vcc", "memory");
  if (__builtin_expect(bad, 0)) {
    asm volatile("s_waitcnt vmcnt(0)" ::: "memory");
    const int lane = tid & 63;
    const int wid = __builtin_amdgcn_readfirstlane(tid >> 6);
    const int bid = blockIdx.x, idx8 = bid >> 3;
    const int head = (bid & 7) * 8 + (idx8 >> 2), row0 = (idx8 & 3) * 512 + wid * 64;
    const size_t hb = (size_t)head * SEQ * DH;
    const lds_ptr stg = (lds_ptr)lds + LDS_FB + wid * 4096;
#pragma unroll 1
    for (int b = 0; b < 2; ++b) fallback_block(Q + hb + (size_t)(row0 + 32 * b) * DH, K + hb, V + hb, O + hb + (size_t)(row0 + 32 * b) * DH, stg, lane);
  }
}

extern "C" void kernel_launch(void* const* d_in, const int* in_sizes, int n_in, void* d_out, int out_size, void* d_ws, size_t ws_size, hipStream_t stream) {
  (void)in_sizes; (void)n_in; (void)out_size; (void)d_ws; (void)ws_size;
  hipLaunchKernelGGL(attn_d32_kernel, dim3(256), dim3(512), 0, stream, (const unsigned short*)d_in[0], (const unsigned short*)d_in[1], (const unsigned short*)d_in[2], (unsigned short*)d_out);
}
```
